```python
import torch

def _softmax(x, axis=-1):
    return torch.softmax(x, dim=axis)
def _gelu(x):
    return torch.nn.functional.gelu(x, approximate="tanh")
def _silu(x):
    return torch.nn.functional.silu(x)
def _sigmoid(x):
    return torch.sigmoid(x)
def _layernorm(x, axis=-1, eps=1e-5):
    m = x.mean(dim=axis, keepdim=True)
    v = x.var(dim=axis, keepdim=True, unbiased=False)
    return (x - m) / torch.sqrt(v + eps)
def _scatter_add(tgt, idx, src, axis=0):
    return tgt.clone().index_add_(axis, idx, src)
def _segment_sum(src, seg, n):
    out = torch.zeros((n,) + src.shape[1:], dtype=src.dtype)
    return out.index_add_(0, seg, src)
def _pad(x, pw):
    flat = tuple(v for pair in reversed(pw) for v in pair)
    return torch.nn.functional.pad(x, flat)

kernel_name = 'parallel_sum__grouped_kv_mqa__glu_gated_bilinear__prompt'

def setup_inputs():

    _w = (lambda a: (lambda t: t.to(torch.bfloat16) if t.is_floating_point() else t)(torch.as_tensor(a)))
    def _A_setup():
        B, S, D, H = 1, 2048, 448, 16
        Dh = D // H
        return {
            "x": _w((lambda *s: torch.randn(*s, dtype=torch.bfloat16) * 0.05)(B, S, D)),
            "wq": _w((lambda *s: torch.randn(*s, dtype=torch.bfloat16) * 0.05)(D, H * Dh)),
            "wk": _w((lambda *s: torch.randn(*s, dtype=torch.bfloat16) * 0.05)(D, Dh)),
            "wv": _w((lambda *s: torch.randn(*s, dtype=torch.bfloat16) * 0.05)(D, Dh)),
            "wo": _w((lambda *s: torch.randn(*s, dtype=torch.bfloat16) * 0.05)(H * Dh, D)),
        }
    _A_in = _A_setup()

    def _B_setup():
        B, S, D, F = 1, 2048, 448, 1792
        return {
            "x": _w((lambda *s: torch.randn(*s, dtype=torch.bfloat16) * 0.05)(B, S, D)),
            "w_gate": _w((lambda *s: torch.randn(*s, dtype=torch.bfloat16) * 0.4)(D, F) * (D ** -0.5)),
            "w_up": _w((lambda *s: torch.randn(*s, dtype=torch.bfloat16) * 0.4)(D, F) * (D ** -0.5)),
            "w_down": _w((lambda *s: torch.randn(*s, dtype=torch.bfloat16) * 0.4)(F, D) * (F ** -0.5)),
        }
    _B_in = _B_setup()

    return {
        "A_x": _A_in["x"],
        "A_wq": _A_in["wq"],
        "A_wk": _A_in["wk"],
        "A_wv": _A_in["wv"],
        "A_wo": _A_in["wo"],
        "B_x": _B_in["x"],
        "B_w_gate": _B_in["w_gate"],
        "B_w_up": _B_in["w_up"],
        "B_w_down": _B_in["w_down"],
    }

def reference(A_x, A_wq, A_wk, A_wv, A_wo, B_x, B_w_gate, B_w_up, B_w_down):
    def _A_ref(x, wq, wk, wv, wo):
        B, S, D = x.shape
        H = 16
        Dh = D // H
        q = (lambda x, *d: x.permute(*d))((x @ wq).reshape(B, S, H, Dh), 0, 2, 1, 3)
        k = x @ wk
        v = x @ wv
        scores = torch.einsum("bhqd,bkd->bhqk", q, k) * (Dh ** -0.5)
        p = _softmax(scores, axis=-1)
        o = torch.einsum("bhqk,bkd->bhqd", p, v)
        return (lambda x, *d: x.permute(*d))(o, 0, 2, 1, 3).reshape(B, S, H * Dh) @ wo

    def _B_ref(x, w_gate, w_up, w_down):
        g = torch.einsum("bsd,df->bsf", x, w_gate)
        u = torch.einsum("bsd,df->bsf", x, w_up)
        h = g * u
        return torch.einsum("bsf,fd->bsd", h, w_down)

    x = A_x
    xn = _layernorm(x, axis=-1)
    return x + _A_ref(xn, A_wq, A_wk, A_wv, A_wo) + _B_ref(xn, B_w_gate, B_w_up, B_w_down)
```

```cpp
#include <hip/hip_runtime.h>
#include <hip/hip_bf16.h>
#include <stdint.h>

typedef float f32x16 __attribute__((ext_vector_type(16)));
typedef float f32x4 __attribute__((ext_vector_type(4)));
typedef float f32x2 __attribute__((ext_vector_type(2)));
typedef __bf16 bf16x8 __attribute__((ext_vector_type(8)));
typedef __bf16 bf16x2 __attribute__((ext_vector_type(2)));
typedef unsigned u32x4 __attribute__((ext_vector_type(4)));
typedef unsigned u32x2 __attribute__((ext_vector_type(2)));
typedef unsigned short u16;
#define DI __device__ __forceinline__
#define MFMA(a, b, c) __builtin_amdgcn_mfma_f32_32x32x16_bf16(__builtin_bit_cast(bf16x8, a), __builtin_bit_cast(bf16x8, b), c, 0, 0, 0)

constexpr int S = 2048, D = 448, H = 16, DH = 28, F = 1792;
constexpr size_t WS_Q = 0;
constexpr size_t WS_KV = WS_Q + 2097152;
constexpr size_t WS_H = WS_KV + 262144;
constexpr size_t WS_O = WS_H + 7340032;
constexpr size_t WS_WD = WS_O + 2097152;
constexpr size_t WS_WO = WS_WD + 14 * 112 * 1024;
constexpr size_t WS_KN = WS_WO + 14 * 32 * 1024;

DI unsigned cvtpk(float lo, float hi) { f32x2 v = {lo, hi}; bf16x2 b = __builtin_convertvector(v, bf16x2); return __builtin_bit_cast(unsigned, b); }
DI float bflo(unsigned p) { return __uint_as_float(p << 16); }
DI float bfhi(unsigned p) { return __uint_as_float(p & 0xffff0000u); }
DI float swap_add(float v) { auto r = __builtin_amdgcn_permlane32_swap(__float_as_uint(v), __float_as_uint(v), false, false); return __uint_as_float(r[0]) + __uint_as_float(r[1]); }
DI float swap_max(float v) { auto r = __builtin_amdgcn_permlane32_swap(__float_as_uint(v), __float_as_uint(v), false, false); return fmaxf(__uint_as_float(r[0]), __uint_as_float(r[1])); }

__global__ __launch_bounds__(256) void k_p1(const u16* __restrict__ x, const u16* __restrict__ wq, const u16* __restrict__ wk, const u16* __restrict__ wv,
                                            const u16* __restrict__ wg, const u16* __restrict__ wu, const u16* __restrict__ wd, const u16* __restrict__ wo, char* ws) {
  const int lane = threadIdx.x & 63, w = __builtin_amdgcn_readfirstlane(threadIdx.x >> 6), r = lane & 31, h = lane >> 5;
  const int tt = blockIdx.x & 63, cq = blockIdx.x >> 6;
  {
    int fi = blockIdx.x * 4 + w;
    if (fi < 2016) {
      int dt = fi / 144, ks = fi % 144;
      u16 e[8];
      for (int j = 0; j < 8; ++j) {
        int kk = 8 * (j >> 2) + 4 * h + (j & 3);
        if (ks < 112) e[j] = wd[(size_t)(16 * ks + kk) * D + 32 * dt + r];
        else { int k2 = ks - 112, hd = k2 >> 1, dim = 16 * (k2 & 1) + kk; e[j] = dim < DH ? wo[(size_t)(DH * hd + dim) * D + 32 * dt + r] : (u16)0; }
      }
      u32x4 v; for (int j = 0; j < 4; ++j) v[j] = e[2 * j] | ((unsigned)e[2 * j + 1] << 16);
      char* dst = ks < 112 ? ws + WS_WD + (size_t)(dt * 112 + ks) * 1024 : ws + WS_WO + (size_t)(dt * 32 + ks - 112) * 1024;
      *(u32x4*)(dst + lane * 16) = v;
    }
  }
  u32x4 xf[28];
  float s1 = 0.f;
#pragma unroll
  for (int s = 0; s < 28; ++s) {
    xf[s] = *(const u32x4*)(x + (size_t)(tt * 32 + r) * D + 16 * s + 8 * h);
#pragma unroll
    for (int j = 0; j < 4; ++j) s1 += bflo(xf[s][j]) + bfhi(xf[s][j]);
  }
  const float mean = swap_add(s1) * (1.f / D);
  float s2 = 0.f;
#pragma unroll
  for (int s = 0; s < 28; ++s)
#pragma unroll
    for (int j = 0; j < 4; ++j) { float a = bflo(xf[s][j]) - mean, b = bfhi(xf[s][j]) - mean; s2 += a * a + b * b; }
  const float rstd = rsqrtf(swap_add(s2) * (1.f / D) + 1e-5f);
#pragma unroll
  for (int s = 0; s < 28; ++s)
#pragma unroll
    for (int j = 0; j < 4; ++j) xf[s][j] = cvtpk((bflo(xf[s][j]) - mean) * rstd, (bfhi(xf[s][j]) - mean) * rstd);

  for (int ct = cq * 4 + w; ct < 130; ct += 32) {
    const u16* wp; int ld; bool valid = true;
    if (ct < 16) { wp = wq + DH * ct + (r < DH ? r : 0); ld = D; valid = r < DH; }
    else if (ct == 16) { wp = wk + (r < DH ? r : 0); ld = DH; valid = r < DH; }
    else if (ct == 17) { wp = wv + (r < DH ? r : 0); ld = DH; valid = r < DH; }
    else { wp = (r < 16 ? wg : wu) + 16 * (ct - 18) + (r & 15); ld = F; }
    f32x16 acc = {};
#pragma unroll
    for (int s = 0; s < 28; ++s) {
      u32x4 a;
#pragma unroll
      for (int j = 0; j < 4; ++j) {
        unsigned lo = wp[(16 * s + 8 * h + 2 * j) * ld], hi = wp[(16 * s + 8 * h + 2 * j + 1) * ld];
        a[j] = valid ? lo | (hi << 16) : 0;
      }
      if (ct == 17) acc = MFMA(xf[s], a, acc); else acc = MFMA(a, xf[s], acc);
    }
    if (ct < 16) {
      const float c = 0.18898223650461362f * 1.4426950408889634f;
      char* dst = ws + WS_Q + (size_t)((((tt >> 2) * 16 + ct) * 4 + (tt & 3)) * 2) * 1024 + lane * 16;
      for (int s = 0; s < 2; ++s) { u32x4 v; for (int j = 0; j < 4; ++j) v[j] = cvtpk(acc[8 * s + 2 * j] * c, acc[8 * s + 2 * j + 1] * c); *(u32x4*)(dst + s * 1024) = v; }
    } else if (ct == 16) {
      u32x4 v[2]; float n2 = 0.f;
      for (int s = 0; s < 2; ++s) for (int j = 0; j < 4; ++j) { v[s][j] = cvtpk(acc[8 * s + 2 * j], acc[8 * s + 2 * j + 1]); float a = bflo(v[s][j]), b = bfhi(v[s][j]); n2 += a * a + b * b; }
      n2 = swap_add(n2);
      for (int o = 1; o < 32; o <<= 1) n2 = fmaxf(n2, __shfl_xor(n2, o));
      if (h == 1) { v[1][2] = 0x3f803f80u; v[1][3] = 0x00003f80u; }
      char* dst = ws + WS_KV + (size_t)tt * 4096 + lane * 16;
      *(u32x4*)dst = v[0]; *(u32x4*)(dst + 1024) = v[1];
      if (lane == 0) ((float*)(ws + WS_KN))[tt] = n2;
    } else if (ct == 17) {
      char* dst = ws + WS_KV + (size_t)tt * 4096 + 2048 + lane * 16;
      for (int s = 0; s < 2; ++s) { u32x4 v; for (int j = 0; j < 4; ++j) v[j] = r == 28 ? 0x3f803f80u : cvtpk(acc[8 * s + 2 * j], acc[8 * s + 2 * j + 1]); *(u32x4*)(dst + s * 1024) = v; }
    } else {
      u32x4 v; for (int j = 0; j < 4; ++j) v[j] = cvtpk(acc[2 * j] * acc[8 + 2 * j], acc[2 * j + 1] * acc[8 + 2 * j + 1]);
      *(u32x4*)(ws + WS_H + (size_t)(tt * 112 + ct - 18) * 1024 + lane * 16) = v;
    }
  }
}

__global__ __launch_bounds__(256, 2) void k_p2(char* ws) {
  __shared__ __attribute__((aligned(16))) float red[4 * 3 * 1024];
  const int lane = threadIdx.x & 63, w = __builtin_amdgcn_readfirstlane(threadIdx.x >> 6), h = lane >> 5;
  const int rb = blockIdx.x >> 4, hd = blockIdx.x & 15;
  u32x4 q[4][2];
  const char* qp = ws + WS_Q + (size_t)blockIdx.x * 8192 + lane * 16;
#pragma unroll
  for (int t = 0; t < 4; ++t) { q[t][0] = *(const u32x4*)(qp + t * 2048); q[t][1] = *(const u32x4*)(qp + t * 2048 + 1024); }
  float kn2 = ((const float*)(ws + WS_KN))[lane];
  const char* kvp = ws + WS_KV + (size_t)w * 65536 + lane * 16;
  for (int o = 1; o < 64; o <<= 1) kn2 = fmaxf(kn2, __shfl_xor(kn2, o));
  float shift[4]; bool bad = false;
#pragma unroll
  for (int t = 0; t < 4; ++t) {
    if (h == 1) { q[t][1][2] = 0; q[t][1][3] = 0; }
    float n2 = 0.f;
#pragma unroll
    for (int s = 0; s < 2; ++s)
#pragma unroll
      for (int j = 0; j < 4; ++j) { float a = bflo(q[t][s][j]), b = bfhi(q[t][s][j]); n2 += a * a + b * b; }
    n2 = swap_add(n2);
    shift[t] = sqrtf(n2 * kn2);
    bad |= !(shift[t] <= 40.f);
  }
  if (__any(bad)) {
    float m[4] = {-INFINITY, -INFINITY, -INFINITY, -INFINITY};
    for (int kt = 0; kt < 16; ++kt) {
      u32x4 k0 = *(const u32x4*)(kvp + kt * 4096), k1 = *(const u32x4*)(kvp + kt * 4096 + 1024);
#pragma unroll
      for (int t = 0; t < 4; ++t) {
        f32x16 s = {}; s = MFMA(k0, q[t][0], s); s = MFMA(k1, q[t][1], s);
#pragma unroll
        for (int i = 0; i < 16; ++i) m[t] = fmaxf(m[t], s[i]);
      }
    }
#pragma unroll
    for (int t = 0; t < 4; ++t) red[(w * 4 + t) * 64 + lane] = swap_max(m[t]);
    __syncthreads();
#pragma unroll
    for (int t = 0; t < 4; ++t) shift[t] = fmaxf(fmaxf(red[t * 64 + lane], red[(4 + t) * 64 + lane]), fmaxf(red[(8 + t) * 64 + lane], red[(12 + t) * 64 + lane]));
    __syncthreads();
  }
#pragma unroll
  for (int t = 0; t < 4; ++t) {
    float a = -shift[t];
    unsigned p0 = cvtpk(a, 0.f) & 0xffff; float r1 = a - bflo(p0);
    unsigned p1 = cvtpk(r1, 0.f) & 0xffff; float r2 = r1 - bflo(p1);
    unsigned p2 = cvtpk(r2, 0.f) & 0xffff;
    if (h == 1) { q[t][1][2] = p0 | (p1 << 16); q[t][1][3] = p2; }
  }
  f32x16 o[4] = {};
  u32x4 k0 = *(const u32x4*)(kvp), k1 = *(const u32x4*)(kvp + 1024), v0 = *(const u32x4*)(kvp + 2048), v1 = *(const u32x4*)(kvp + 3072);
  for (int kt = 0; kt < 16; ++kt) {
    const int kn = kt < 15 ? kt + 1 : kt;
    u32x4 nk0 = *(const u32x4*)(kvp + kn * 4096), nk1 = *(const u32x4*)(kvp + kn * 4096 + 1024), nv0 = *(const u32x4*)(kvp + kn * 4096 + 2048), nv1 = *(const u32x4*)(kvp + kn * 4096 + 3072);
#pragma unroll
    for (int t = 0; t < 4; ++t) {
      f32x16 s = {}; s = MFMA(k0, q[t][0], s); s = MFMA(k1, q[t][1], s);
      u32x4 p0, p1;
#pragma unroll
      for (int j = 0; j < 4; ++j) {
        p0[j] = cvtpk(__builtin_amdgcn_exp2f(s[2 * j]), __builtin_amdgcn_exp2f(s[2 * j + 1]));
        p1[j] = cvtpk(__builtin_amdgcn_exp2f(s[8 + 2 * j]), __builtin_amdgcn_exp2f(s[8 + 2 * j + 1]));
      }
      o[t] = MFMA(v0, p0, o[t]); o[t] = MFMA(v1, p1, o[t]);
    }
    k0 = nk0; k1 = nk1; v0 = nv0; v1 = nv1;
  }
#pragma unroll
  for (int t = 0; t < 4; ++t) {
    if (t != w) {
      const int slot = t * 3 + (w > t ? w - 1 : w);
#pragma unroll
      for (int g = 0; g < 4; ++g) *(f32x4*)(red + slot * 1024 + g * 256 + lane * 4) = f32x4{o[t][4 * g], o[t][4 * g + 1], o[t][4 * g + 2], o[t][4 * g + 3]};
    }
  }
  __syncthreads();
  f32x16 m = w == 0 ? o[0] : w == 1 ? o[1] : w == 2 ? o[2] : o[3];
#pragma unroll
  for (int u = 0; u < 3; ++u)
#pragma unroll
    for (int g = 0; g < 4; ++g) { f32x4 v = *(const f32x4*)(red + (w * 3 + u) * 1024 + g * 256 + lane * 4); for (int i = 0; i < 4; ++i) m[4 * g + i] += v[i]; }
  auto lr = __builtin_amdgcn_permlane32_swap(__float_as_uint(m[12]), __float_as_uint(m[12]), false, false);
  const float inv = 1.f / __uint_as_float(lr[1]);
  char* dst = ws + WS_O + (size_t)((rb * 4 + w) * 32 + hd * 2) * 1024 + lane * 16;
#pragma unroll
  for (int s = 0; s < 2; ++s) { u32x4 v; for (int j = 0; j < 4; ++j) v[j] = cvtpk(m[8 * s + 2 * j] * inv, m[8 * s + 2 * j + 1] * inv); *(u32x4*)(dst + s * 1024) = v; }
}

__global__ __launch_bounds__(256, 2) void k_p3(const char* ws, const u16* __restrict__ x, u16* __restrict__ out) {
  __shared__ __attribute__((aligned(16))) float red[4 * 3 * 1024];
  const int lane = threadIdx.x & 63, w = __builtin_amdgcn_readfirstlane(threadIdx.x >> 6), r = lane & 31, h = lane >> 5;
  const int g8 = blockIdx.x & 7, i8 = blockIdx.x >> 3;
  const int tb = g8 * 4 + i8 / 7, cb = i8 % 7;
  f32x16 acc[2][2] = {};
  const char* hp = ws + WS_H + (size_t)(tb * 2 * 112 + w * 28) * 1024 + lane * 16;
  const char* wp = ws + WS_WD + (size_t)(cb * 2 * 112 + w * 28) * 1024 + lane * 16;
#pragma unroll 7
  for (int ks = 0; ks < 28; ++ks) {
    u32x4 a0 = *(const u32x4*)(wp + ks * 1024), a1 = *(const u32x4*)(wp + (112 + ks) * 1024);
    u32x4 b0 = *(const u32x4*)(hp + ks * 1024), b1 = *(const u32x4*)(hp + (112 + ks) * 1024);
    acc[0][0] = MFMA(a0, b0, acc[0][0]); acc[0][1] = MFMA(a0, b1, acc[0][1]); acc[1][0] = MFMA(a1, b0, acc[1][0]); acc[1][1] = MFMA(a1, b1, acc[1][1]);
  }
  const char* op = ws + WS_O + (size_t)(tb * 2 * 32 + w * 8) * 1024 + lane * 16;
  const char* wop = ws + WS_WO + (size_t)(cb * 2 * 32 + w * 8) * 1024 + lane * 16;
#pragma unroll
  for (int ks = 0; ks < 8; ++ks) {
    u32x4 a0 = *(const u32x4*)(wop + ks * 1024), a1 = *(const u32x4*)(wop + (32 + ks) * 1024);
    u32x4 b0 = *(const u32x4*)(op + ks * 1024), b1 = *(const u32x4*)(op + (32 + ks) * 1024);
    acc[0][0] = MFMA(a0, b0, acc[0][0]); acc[0][1] = MFMA(a0, b1, acc[0][1]); acc[1][0] = MFMA(a1, b0, acc[1][0]); acc[1][1] = MFMA(a1, b1, acc[1][1]);
  }
#pragma unroll
  for (int t = 0; t < 4; ++t) {
    if (t != w) {
      const int slot = t * 3 + (w > t ? w - 1 : w);
      const f32x16 a = acc[t >> 1][t & 1];
#pragma unroll
      for (int g = 0; g < 4; ++g) *(f32x4*)(red + slot * 1024 + g * 256 + lane * 4) = f32x4{a[4 * g], a[4 * g + 1], a[4 * g + 2], a[4 * g + 3]};
    }
  }
  __syncthreads();
  f32x16 m = w == 0 ? acc[0][0] : w == 1 ? acc[0][1] : w == 2 ? acc[1][0] : acc[1][1];
#pragma unroll
  for (int u = 0; u < 3; ++u)
#pragma unroll
    for (int g = 0; g < 4; ++g) { f32x4 v = *(const f32x4*)(red + (w * 3 + u) * 1024 + g * 256 + lane * 4); for (int i = 0; i < 4; ++i) m[4 * g + i] += v[i]; }
  const size_t base = (size_t)(tb * 64 + (w & 1) * 32 + r) * D + cb * 64 + (w >> 1) * 32 + 4 * h;
#pragma unroll
  for (int g = 0; g < 4; ++g) {
    u32x2 xv = *(const u32x2*)(x + base + 8 * g);
    u32x2 ov = {cvtpk(m[4 * g] + bflo(xv[0]), m[4 * g + 1] + bfhi(xv[0])), cvtpk(m[4 * g + 2] + bflo(xv[1]), m[4 * g + 3] + bfhi(xv[1]))};
    *(u32x2*)(out + base + 8 * g) = ov;
  }
}

#define NDUMMY 0
__global__ void k_dummy(char* ws) { if (ws == nullptr) ws[0] = 0; }

extern "C" void kernel_launch(void* const* d_in, const int* in_sizes, int n_in, void* d_out, int out_size, void* d_ws, size_t ws_size, hipStream_t stream) {
  const u16* x = (const u16*)d_in[0];
  char* ws = (char*)d_ws;
  k_p1<<<512, 256, 0, stream>>>(x, (const u16*)d_in[1], (const u16*)d_in[2], (const u16*)d_in[3], (const u16*)d_in[6], (const u16*)d_in[7], (const u16*)d_in[8], (const u16*)d_in[4], ws);
  for (int i = 0; i < NDUMMY; ++i) k_dummy<<<256, 256, 0, stream>>>(ws);
  k_p2<<<256, 256, 0, stream>>>(ws);
  k_p3<<<224, 256, 0, stream>>>(ws, x, (u16*)d_out);
}
```

```cpp
#define MAP 0
#define STAT 0
#include <hip/hip_runtime.h>
#include <hip/hip_bf16.h>
#include <stdint.h>

typedef float f32x16 __attribute__((ext_vector_type(16)));
typedef float f32x4 __attribute__((ext_vector_type(4)));
typedef float f32x2 __attribute__((ext_vector_type(2)));
typedef __bf16 bf16x8 __attribute__((ext_vector_type(8)));
typedef __bf16 bf16x2 __attribute__((ext_vector_type(2)));
typedef unsigned u32x4 __attribute__((ext_vector_type(4)));
typedef unsigned u32x2 __attribute__((ext_vector_type(2)));
typedef unsigned short u16;
#define DI __device__ __forceinline__
#define MFMA(a, b, c) __builtin_amdgcn_mfma_f32_32x32x16_bf16(__builtin_bit_cast(bf16x8, a), __builtin_bit_cast(bf16x8, b), c, 0, 0, 0)

constexpr int S = 2048, D = 448, H = 16, DH = 28, F = 1792;
constexpr size_t WS_Q = 0;
constexpr size_t WS_KV = WS_Q + 2097152;
constexpr size_t WS_H = WS_KV + 262144;
constexpr size_t WS_O = WS_H + 7340032;
constexpr size_t WS_WD = WS_O + 2097152;
constexpr size_t WS_WO = WS_WD + 14 * 112 * 1024;
constexpr size_t WS_KN = WS_WO + 14 * 32 * 1024;

DI unsigned cvtpk(float lo, float hi) { f32x2 v = {lo, hi}; bf16x2 b = __builtin_convertvector(v, bf16x2); return __builtin_bit_cast(unsigned, b); }
DI float bflo(unsigned p) { return __uint_as_float(p << 16); }
DI float bfhi(unsigned p) { return __uint_as_float(p & 0xffff0000u); }
DI float swap_add(float v) { auto r = __builtin_amdgcn_permlane32_swap(__float_as_uint(v), __float_as_uint(v), false, false); return __uint_as_float(r[0]) + __uint_as_float(r[1]); }
DI float swap_max(float v) { auto r = __builtin_amdgcn_permlane32_swap(__float_as_uint(v), __float_as_uint(v), false, false); return fmaxf(__uint_as_float(r[0]), __uint_as_float(r[1])); }

__global__ __launch_bounds__(256) void k_p1(const u16* __restrict__ x, const u16* __restrict__ wq, const u16* __restrict__ wk, const u16* __restrict__ wv,
                                            const u16* __restrict__ wg, const u16* __restrict__ wu, const u16* __restrict__ wd, const u16* __restrict__ wo, char* ws) {
  const int lane = threadIdx.x & 63, w = __builtin_amdgcn_readfirstlane(threadIdx.x >> 6), r = lane & 31, h = lane >> 5;
  const int tt = blockIdx.x & 63, cq = blockIdx.x >> 6;
  {
    int fi = blockIdx.x * 4 + w;
    if (fi < 2016) {
      int dt = fi / 144, ks = fi % 144;
      u16 e[8];
      for (int j = 0; j < 8; ++j) {
        int kk = 8 * (j >> 2) + 4 * h + (j & 3);
        if (ks < 112) e[j] = wd[(size_t)(16 * ks + kk) * D + 32 * dt + r];
        else { int k2 = ks - 112, hd = k2 >> 1, dim = 16 * (k2 & 1) + kk; e[j] = dim < DH ? wo[(size_t)(DH * hd + dim) * D + 32 * dt + r] : (u16)0; }
      }
      u32x4 v; for (int j = 0; j < 4; ++j) v[j] = e[2 * j] | ((unsigned)e[2 * j + 1] << 16);
      char* dst = ks < 112 ? ws + WS_WD + (size_t)(dt * 112 + ks) * 1024 : ws + WS_WO + (size_t)(dt * 32 + ks - 112) * 1024;
      *(u32x4*)(dst + lane * 16) = v;
    }
  }
  u32x4 xf[28];
  float s1 = 0.f;
#pragma unroll
  for (int s = 0; s < 28; ++s) {
    xf[s] = *(const u32x4*)(x + (size_t)(tt * 32 + r) * D + 16 * s + 8 * h);
#pragma unroll
    for (int j = 0; j < 4; ++j) s1 += bflo(xf[s][j]) + bfhi(xf[s][j]);
  }
  const float mean = swap_add(s1) * (1.f / D);
  float s2 = 0.f;
#pragma unroll
  for (int s = 0; s < 28; ++s)
#pragma unroll
    for (int j = 0; j < 4; ++j) { float a = bflo(xf[s][j]) - mean, b = bfhi(xf[s][j]) - mean; s2 += a * a + b * b; }
  const float rstd = rsqrtf(swap_add(s2) * (1.f / D) + 1e-5f);
#pragma unroll
  for (int s = 0; s < 28; ++s)
#pragma unroll
    for (int j = 0; j < 4; ++j) xf[s][j] = cvtpk((bflo(xf[s][j]) - mean) * rstd, (bfhi(xf[s][j]) - mean) * rstd);

  for (int ct = cq * 4 + w; ct < 130; ct += 32) {
    const u16* wp; int ld; bool valid = true;
    if (ct < 16) { wp = wq + DH * ct + (r < DH ? r : 0); ld = D; valid = r < DH; }
    else if (ct == 16) { wp = wk + (r < DH ? r : 0); ld = DH; valid = r < DH; }
    else if (ct == 17) { wp = wv + (r < DH ? r : 0); ld = DH; valid = r < DH; }
    else { wp = (r < 16 ? wg : wu) + 16 * (ct - 18) + (r & 15); ld = F; }
    f32x16 acc = {};
#pragma unroll
    for (int s = 0; s < 28; ++s) {
      u32x4 a;
#pragma unroll
      for (int j = 0; j < 4; ++j) {
        unsigned lo = wp[(16 * s + 8 * h + 2 * j) * ld], hi = wp[(16 * s + 8 * h + 2 * j + 1) * ld];
        a[j] = valid ? lo | (hi << 16) : 0;
      }
      if (ct == 17) acc = MFMA(xf[s], a, acc); else acc = MFMA(a, xf[s], acc);
    }
    if (ct < 16) {
      const float c = 0.18898223650461362f * 1.4426950408889634f;
      char* dst = ws + WS_Q + (size_t)((((tt >> 2) * 16 + ct) * 4 + (tt & 3)) * 2) * 1024 + lane * 16;
      for (int s = 0; s < 2; ++s) { u32x4 v; for (int j = 0; j < 4; ++j) v[j] = cvtpk(acc[8 * s + 2 * j] * c, acc[8 * s + 2 * j + 1] * c); *(u32x4*)(dst + s * 1024) = v; }
    } else if (ct == 16) {
      u32x4 v[2]; float n2 = 0.f;
      for (int s = 0; s < 2; ++s) for (int j = 0; j < 4; ++j) { v[s][j] = cvtpk(acc[8 * s + 2 * j], acc[8 * s + 2 * j + 1]); float a = bflo(v[s][j]), b = bfhi(v[s][j]); n2 += a * a + b * b; }
      n2 = swap_add(n2);
      for (int o = 1; o < 32; o <<= 1) n2 = fmaxf(n2, __shfl_xor(n2, o));
      if (h == 1) { v[1][2] = 0x3f803f80u; v[1][3] = 0x00003f80u; }
      char* dst = ws + WS_KV + (size_t)tt * 4096 + lane * 16;
      *(u32x4*)dst = v[0]; *(u32x4*)(dst + 1024) = v[1];
      if (lane == 0) ((float*)(ws + WS_KN))[tt] = n2;
    } else if (ct == 17) {
      char* dst = ws + WS_KV + (size_t)tt * 4096 + 2048 + lane * 16;
      for (int s = 0; s < 2; ++s) { u32x4 v; for (int j = 0; j < 4; ++j) v[j] = r == 28 ? 0x3f803f80u : cvtpk(acc[8 * s + 2 * j], acc[8 * s + 2 * j + 1]); *(u32x4*)(dst + s * 1024) = v; }
    } else {
      u32x4 v; for (int j = 0; j < 4; ++j) v[j] = cvtpk(acc[2 * j] * acc[8 + 2 * j], acc[2 * j + 1] * acc[8 + 2 * j + 1]);
      *(u32x4*)(ws + WS_H + (size_t)(tt * 112 + ct - 18) * 1024 + lane * 16) = v;
    }
  }
}

__global__ __launch_bounds__(256, 2) void k_p2(char* ws) {
  __shared__ __attribute__((aligned(16))) float red[4 * 3 * 1024];
  const int lane = threadIdx.x & 63, w = __builtin_amdgcn_readfirstlane(threadIdx.x >> 6), h = lane >> 5;
  const int rb = blockIdx.x >> 4, hd = blockIdx.x & 15;
  u32x4 q[4][2];
  const char* qp = ws + WS_Q + (size_t)blockIdx.x * 8192 + lane * 16;
#pragma unroll
  for (int t = 0; t < 4; ++t) { q[t][0] = *(const u32x4*)(qp + t * 2048); q[t][1] = *(const u32x4*)(qp + t * 2048 + 1024); }
  float kn2 = ((const float*)(ws + WS_KN))[lane];
  const char* kvp = ws + WS_KV + (size_t)w * 65536 + lane * 16;
  for (int o = 1; o < 64; o <<= 1) kn2 = fmaxf(kn2, __shfl_xor(kn2, o));
  float shift[4]; bool bad = false;
#pragma unroll
  for (int t = 0; t < 4; ++t) {
    if (h == 1) { q[t][1][2] = 0; q[t][1][3] = 0; }
    float n2 = 0.f;
#pragma unroll
    for (int s = 0; s < 2; ++s)
#pragma unroll
      for (int j = 0; j < 4; ++j) { float a = bflo(q[t][s][j]), b = bfhi(q[t][s][j]); n2 += a * a + b * b; }
    n2 = swap_add(n2);
    shift[t] = sqrtf(n2 * kn2);
    bad |= !(shift[t] <= 40.f);
  }
  if (__any(bad)) {
    float m[4] = {-INFINITY, -INFINITY, -INFINITY, -INFINITY};
    for (int kt = 0; kt < 16; ++kt) {
      u32x4 k0 = *(const u32x4*)(kvp + kt * 4096), k1 = *(const u32x4*)(kvp + kt * 4096 + 1024);
#pragma unroll
      for (int t = 0; t < 4; ++t) {
        f32x16 s = {}; s = MFMA(k0, q[t][0], s); s = MFMA(k1, q[t][1], s);
#pragma unroll
        for (int i = 0; i < 16; ++i) m[t] = fmaxf(m[t], s[i]);
      }
    }
#pragma unroll
    for (int t = 0; t < 4; ++t) red[(w * 4 + t) * 64 + lane] = swap_max(m[t]);
    __syncthreads();
#pragma unroll
    for (int t = 0; t < 4; ++t) shift[t] = fmaxf(fmaxf(red[t * 64 + lane], red[(4 + t) * 64 + lane]), fmaxf(red[(8 + t) * 64 + lane], red[(12 + t) * 64 + lane]));
    __syncthreads();
  }
#pragma unroll
  for (int t = 0; t < 4; ++t) {
    float a = -shift[t];
    unsigned p0 = cvtpk(a, 0.f) & 0xffff; float r1 = a - bflo(p0);
    unsigned p1 = cvtpk(r1, 0.f) & 0xffff; float r2 = r1 - bflo(p1);
    unsigned p2 = cvtpk(r2, 0.f) & 0xffff;
    if (h == 1) { q[t][1][2] = p0 | (p1 << 16); q[t][1][3] = p2; }
  }
  f32x16 o[4] = {};
  u32x4 k0 = *(const u32x4*)(kvp), k1 = *(const u32x4*)(kvp + 1024), v0 = *(const u32x4*)(kvp + 2048), v1 = *(const u32x4*)(kvp + 3072);
  for (int kt = 0; kt < 16; ++kt) {
    const int kn = kt < 15 ? kt + 1 : kt;
    u32x4 nk0 = *(const u32x4*)(kvp + kn * 4096), nk1 = *(const u32x4*)(kvp + kn * 4096 + 1024), nv0 = *(const u32x4*)(kvp + kn * 4096 + 2048), nv1 = *(const u32x4*)(kvp + kn * 4096 + 3072);
#pragma unroll
    for (int t = 0; t < 4; ++t) {
      f32x16 s = {}; s = MFMA(k0, q[t][0], s); s = MFMA(k1, q[t][1], s);
      u32x4 p0, p1;
#pragma unroll
      for (int j = 0; j < 4; ++j) {
        p0[j] = cvtpk(__builtin_amdgcn_exp2f(s[2 * j]), __builtin_amdgcn_exp2f(s[2 * j + 1]));
        p1[j] = cvtpk(__builtin_amdgcn_exp2f(s[8 + 2 * j]), __builtin_amdgcn_exp2f(s[8 + 2 * j + 1]));
      }
      o[t] = MFMA(v0, p0, o[t]); o[t] = MFMA(v1, p1, o[t]);
    }
    k0 = nk0; k1 = nk1; v0 = nv0; v1 = nv1;
  }
#pragma unroll
  for (int t = 0; t < 4; ++t) {
    if (t != w) {
      const int slot = t * 3 + (w > t ? w - 1 : w);
#pragma unroll
      for (int g = 0; g < 4; ++g) *(f32x4*)(red + slot * 1024 + g * 256 + lane * 4) = f32x4{o[t][4 * g], o[t][4 * g + 1], o[t][4 * g + 2], o[t][4 * g + 3]};
    }
  }
  __syncthreads();
  f32x16 m = w == 0 ? o[0] : w == 1 ? o[1] : w == 2 ? o[2] : o[3];
#pragma unroll
  for (int u = 0; u < 3; ++u)
#pragma unroll
    for (int g = 0; g < 4; ++g) { f32x4 v = *(const f32x4*)(red + (w * 3 + u) * 1024 + g * 256 + lane * 4); for (int i = 0; i < 4; ++i) m[4 * g + i] += v[i]; }
  auto lr = __builtin_amdgcn_permlane32_swap(__float_as_uint(m[12]), __float_as_uint(m[12]), false, false);
  const float inv = 1.f / __uint_as_float(lr[1]);
  char* dst = ws + WS_O + (size_t)((rb * 4 + w) * 32 + hd * 2) * 1024 + lane * 16;
#pragma unroll
  for (int s = 0; s < 2; ++s) { u32x4 v; for (int j = 0; j < 4; ++j) v[j] = cvtpk(m[8 * s + 2 * j] * inv, m[8 * s + 2 * j + 1] * inv); *(u32x4*)(dst + s * 1024) = v; }
}

__global__ __launch_bounds__(256, 2) void k_p3(const char* ws, const u16* __restrict__ x, u16* __restrict__ out) {
  __shared__ __attribute__((aligned(16))) float red[4 * 3 * 1024];
  const int lane = threadIdx.x & 63, w = __builtin_amdgcn_readfirstlane(threadIdx.x >> 6), r = lane & 31, h = lane >> 5;
  const int g8 = blockIdx.x & 7, i8 = blockIdx.x >> 3;
  const int tb = g8 * 4 + i8 / 7, cb = i8 % 7;
  f32x16 acc[2][2] = {};
  const char* hp = ws + WS_H + (size_t)(tb * 2 * 112 + w * 28) * 1024 + lane * 16;
  const char* wp = ws + WS_WD + (size_t)(cb * 2 * 112 + w * 28) * 1024 + lane * 16;
#pragma unroll 7
  for (int ks = 0; ks < 28; ++ks) {
    u32x4 a0 = *(const u32x4*)(wp + ks * 1024), a1 = *(const u32x4*)(wp + (112 + ks) * 1024);
    u32x4 b0 = *(const u32x4*)(hp + ks * 1024), b1 = *(const u32x4*)(hp + (112 + ks) * 1024);
    acc[0][0] = MFMA(a0, b0, acc[0][0]); acc[0][1] = MFMA(a0, b1, acc[0][1]); acc[1][0] = MFMA(a1, b0, acc[1][0]); acc[1][1] = MFMA(a1, b1, acc[1][1]);
  }
  const char* op = ws + WS_O + (size_t)(tb * 2 * 32 + w * 8) * 1024 + lane * 16;
  const char* wop = ws + WS_WO + (size_t)(cb * 2 * 32 + w * 8) * 1024 + lane * 16;
#pragma unroll
  for (int ks = 0; ks < 8; ++ks) {
    u32x4 a0 = *(const u32x4*)(wop + ks * 1024), a1 = *(const u32x4*)(wop + (32 + ks) * 1024);
    u32x4 b0 = *(const u32x4*)(op + ks * 1024), b1 = *(const u32x4*)(op + (32 + ks) * 1024);
    acc[0][0] = MFMA(a0, b0, acc[0][0]); acc[0][1] = MFMA(a0, b1, acc[0][1]); acc[1][0] = MFMA(a1, b0, acc[1][0]); acc[1][1] = MFMA(a1, b1, acc[1][1]);
  }
#pragma unroll
  for (int t = 0; t < 4; ++t) {
    if (t != w) {
      const int slot = t * 3 + (w > t ? w - 1 : w);
      const f32x16 a = acc[t >> 1][t & 1];
#pragma unroll
      for (int g = 0; g < 4; ++g) *(f32x4*)(red + slot * 1024 + g * 256 + lane * 4) = f32x4{a[4 * g], a[4 * g + 1], a[4 * g + 2], a[4 * g + 3]};
    }
  }
  __syncthreads();
  f32x16 m = w == 0 ? acc[0][0] : w == 1 ? acc[0][1] : w == 2 ? acc[1][0] : acc[1][1];
#pragma unroll
  for (int u = 0; u < 3; ++u)
#pragma unroll
    for (int g = 0; g < 4; ++g) { f32x4 v = *(const f32x4*)(red + (w * 3 + u) * 1024 + g * 256 + lane * 4); for (int i = 0; i < 4; ++i) m[4 * g + i] += v[i]; }
  const size_t base = (size_t)(tb * 64 + (w & 1) * 32 + r) * D + cb * 64 + (w >> 1) * 32 + 4 * h;
#pragma unroll
  for (int g = 0; g < 4; ++g) {
    u32x2 xv = *(const u32x2*)(x + base + 8 * g);
    u32x2 ov = {cvtpk(m[4 * g] + bflo(xv[0]), m[4 * g + 1] + bfhi(xv[0])), cvtpk(m[4 * g + 2] + bflo(xv[1]), m[4 * g + 3] + bfhi(xv[1]))};
    *(u32x2*)(out + base + 8 * g) = ov;
  }
}

constexpr size_t WS_PROBE = 64u << 20;
__global__ __launch_bounds__(256) void k_probe(const char* x, const char* wq, const char* wg, const char* wu, char* ws) {
  extern __shared__ __attribute__((aligned(16))) char smem[];
  const int lane = threadIdx.x & 63, w = __builtin_amdgcn_readfirstlane(threadIdx.x >> 6);
  unsigned long long t0 = __builtin_amdgcn_s_memrealtime();
  const int b = blockIdx.x, g8 = b & 7, i8 = b >> 3;
  const int rb = (g8 & 1) * 8 + (i8 & 7), j = (g8 >> 1) * 4 + (i8 >> 3);
  __amdgpu_buffer_rsrc_t rx = __builtin_amdgcn_make_buffer_rsrc((void*)x, 0, 2048 * 896, 0x00020000);
  __amdgpu_buffer_rsrc_t rg = __builtin_amdgcn_make_buffer_rsrc((void*)wg, 0, 448 * 3584, 0x00020000);
  __amdgpu_buffer_rsrc_t ru = __builtin_amdgcn_make_buffer_rsrc((void*)wu, 0, 448 * 3584, 0x00020000);
  __amdgpu_buffer_rsrc_t rq = __builtin_amdgcn_make_buffer_rsrc((void*)wq, 0, 448 * 896, 0x00020000);
  typedef __attribute__((address_space(3))) void* lp;
  const unsigned xo = (lane >> 3) * 896 + (lane & 7) * 16, wo = (lane >> 4) * 3584 + (lane & 15) * 16, qo = (lane >> 2) * 896 + (lane & 3) * 16;
  const unsigned wcol = (j * 224) & ~63u, qcol = (j * 56) & ~15u;
  unsigned long long t1 = 0, t2 = 0;
#pragma unroll
  for (int c = 0; c < 7; ++c) {
    char* base = smem + w * 39 * 1024 + (c % 3) * 13 * 1024;
    if (c >= 3) asm volatile("s_waitcnt vmcnt(26)" ::: "memory");
    if (c == 3) t1 = __builtin_amdgcn_s_memrealtime();
#pragma unroll
    for (int u = 0; u < 4; ++u) __builtin_amdgcn_raw_ptr_buffer_load_lds(rx, (lp)(base + u * 1024), 16, xo, (rb * 128 + w * 32 + u * 8) * 896 + c * 128, 0, 0);
#pragma unroll
    for (int u = 0; u < 4; ++u) {
      __builtin_amdgcn_raw_ptr_buffer_load_lds(rg, (lp)(base + (4 + u) * 1024), 16, wo, (c * 64 + w * 16 + u * 4) * 3584 + wcol, 0, 0);
      __builtin_amdgcn_raw_ptr_buffer_load_lds(ru, (lp)(base + (8 + u) * 1024), 16, wo, (c * 64 + w * 16 + u * 4) * 3584 + wcol, 0, 0);
    }
    __builtin_amdgcn_raw_ptr_buffer_load_lds(rq, (lp)(base + 12 * 1024), 16, qo, (c * 64 + w * 16) * 896 + qcol, 0, 0);
  }
  asm volatile("s_waitcnt vmcnt(0)" ::: "memory");
  t2 = __builtin_amdgcn_s_memrealtime();
  if (lane == 0) { unsigned long long* p = (unsigned long long*)(ws + WS_PROBE) + (b * 4 + w) * 4; p[0] = t0; p[1] = t1; p[2] = t2; }
}
__global__ __launch_bounds__(1024) void k_amp(char* ws) {
  __shared__ unsigned long long sm[3][1024];
  const unsigned long long* p = (const unsigned long long*)(ws + WS_PROBE) + threadIdx.x * 4;
  sm[0][threadIdx.x] = p[0]; sm[1][threadIdx.x] = p[1]; sm[2][threadIdx.x] = p[2];
  __syncthreads();
  if (threadIdx.x == 0) {
    unsigned long long mn = ~0ull, mx0 = 0, mx1 = 0, mx2 = 0, s1 = 0, s2 = 0;
    for (int i = 0; i < 1024; ++i) { mn = min(mn, sm[0][i]); mx0 = max(mx0, sm[0][i]); mx1 = max(mx1, sm[1][i]); mx2 = max(mx2, sm[2][i]); s1 += sm[1][i] - sm[0][i]; s2 += sm[2][i] - sm[0][i]; }
    unsigned long long stat = STAT == 0 ? 0 : STAT == 1 ? (mx0 - mn) * 100 : STAT == 2 ? s1 * 100 / 1024 : STAT == 3 ? s2 * 100 / 1024 : (mx2 - mn) * 100;
    if (stat > 200000) stat = 200000;
    unsigned long long t = __builtin_amdgcn_s_memrealtime();
    while (__builtin_amdgcn_s_memrealtime() - t < stat) __builtin_amdgcn_s_sleep(1);
  }
}

#define NDUMMY 0
__global__ void k_dummy(char* ws) { if (ws == nullptr) ws[0] = 0; }

extern "C" void kernel_launch(void* const* d_in, const int* in_sizes, int n_in, void* d_out, int out_size, void* d_ws, size_t ws_size, hipStream_t stream) {
  const u16* x = (const u16*)d_in[0];
  char* ws = (char*)d_ws;
  hipFuncSetAttribute((const void*)k_probe, hipFuncAttributeMaxDynamicSharedMemorySize, 160 * 1024);
  k_probe<<<256, 256, 160 * 1024, stream>>>((const char*)d_in[0], (const char*)d_in[1], (const char*)d_in[6], (const char*)d_in[7], ws);
  k_amp<<<1, 1024, 0, stream>>>(ws);
  k_p1<<<512, 256, 0, stream>>>(x, (const u16*)d_in[1], (const u16*)d_in[2], (const u16*)d_in[3], (const u16*)d_in[6], (const u16*)d_in[7], (const u16*)d_in[8], (const u16*)d_in[4], ws);
  for (int i = 0; i < NDUMMY; ++i) k_dummy<<<256, 256, 0, stream>>>(ws);
  k_p2<<<256, 256, 0, stream>>>(ws);
  k_p3<<<224, 256, 0, stream>>>(ws, x, (u16*)d_out);
}
```

```cpp
#include <hip/hip_runtime.h>
#include <hip/hip_bf16.h>

struct Args { const void* p[10]; };

__global__ __attribute__((aligned(4096))) __launch_bounds__(256, 1) void fused_block_kernel(Args args) {
  __shared__ __attribute__((aligned(16))) char lds[163840];
  asm volatile(
    "; ---- set-up\n"
    ".Lcode_0_%=:\n"
    "s_load_dwordx2 s[4:5], s[0:1], 0x0\n"
    "s_mov_b32 s6, 1835008\n"
    "s_mov_b32 s7, 0x00020000\n"
    "; request the code of this kernel into L2 (16 lines per workgroup)\n"
    "s_getpc_b64 s[48:49]\n"
    "s_mov_b32 s50, .Lcode_1_%=-.Lcode_0_%=-512\n"
    "s_mov_b32 s51, s7\n"
    "s_lshr_b32 s52, s2, 3\n"
    "s_lshl_b32 s52, s52, 11\n"
    "v_lshlrev_b32 v159, 3, v0\n"
    "s_mov_b32 exec_lo, 0x00010001\n"
    "s_mov_b32 exec_hi, 0x00010001\n"
    "buffer_load_dword v159, v159, s[48:51], s52 offen\n"
    "s_mov_b64 exec, -1\n"
    "v_lshrrev_b32 v1, 6, v0\n"
    "v_and_b32 v0, 63, v0\n"
    "v_readfirstlane_b32 s68, v1\n"
    "v_lshlrev_b32 v1, 4, v0\n"
    "v_lshrrev_b32 v128, 3, v0\n"
    "v_and_b32 v129, 7, v0\n"
    "v_lshrrev_b32 v130, 4, v0\n"
    "v_xor_b32 v142, v129, v130\n"
    "v_mul_u32_u24 v145, 896, v128\n"
    "v_lshl_add_u32 v134, v142, 4, v145\n"
    "v_xor_b32 v142, 64, v134\n"
    "v_add_u32 v135, 6144, v142\n"
    "s_and_b32 s69, s2, 7\n"
    "s_lshr_b32 s70, s2, 3\n"
    "s_lshr_b32 s88, s2, 5\n"
    "s_and_b32 s79, s69, 3\n"
    "s_and_b32 s80, s70, 3\n"
    "s_lshl2_add_u32 s71, s79, s80\n"
    "s_lshl2_add_u32 s87, s69, s80\n"
    "s_lshl1_add_u32 s79, s80, s88\n"
    "s_cmp_ge_u32 s79, 7\n"
    "s_cselect_b32 s81, 7, 0\n"
    "s_sub_u32 s79, s79, s81\n"
    "s_lshl_b32 s90, s79, 6\n"
    "s_mul_i32 s78, s68, 40960\n"
    "s_lshl2_add_u32 s63, s71, s68\n"
    "s_andn2_b32 s75, 1, s88\n"
    "s_lshl1_add_u32 s75, s63, s75\n"
    "s_mul_i32 s84, s75, 14336\n"
    "s_add_u32 m0, s78, 12288\n"
    "s_lshl1_add_u32 s83, s90, s84\n"
    "s_waitcnt lgkmcnt(0)\n"
    "buffer_load_dwordx4 v134, s[4:7], s83 offen lds\n"
    "buffer_load_dwordx4 v135, s[4:7], s83 offen offset:1024 lds\n"
    "s_add_u32 s91, s90, 64\n"
    "s_cmpk_eq_u32 s91, 448\n"
    "s_cselect_b32 s91, 0, s91\n"
    "s_add_u32 m0, s78, 16384\n"
    "s_lshl1_add_u32 s83, s91, s84\n"
    "buffer_load_dwordx4 v134, s[4:7], s83 offen lds\n"
    "buffer_load_dwordx4 v135, s[4:7], s83 offen offset:1024 lds\n"
    "s_lshr_b32 s79, s69, 2\n"
    "s_lshl3_add_u32 s72, s79, s88\n"
    "s_and_b32 s76, s72, 3\n"
    "s_cmp_eq_u32 s76, 3\n"
    "s_cselect_b32 s89, 1, 0\n"
    "s_cselect_b32 s77, 1, 4\n"
    "s_cselect_b32 s80, 128, 0\n"
    "s_mul_i32 s77, s77, 896\n"
    "s_cmp_eq_u32 s72, 15\n"
    "s_cselect_b32 s89, 2, s89\n"
    "s_cselect_b32 s73, 56, s77\n"
    "s_cselect_b64 s[98:99], -1, 0\n"
    "s_lshl3_add_u32 s52, s89, 48\n"
    "s_cmp_eq_u32 s89, 0\n"
    "s_cselect_b32 s53, 40, 56\n"
    "s_load_dwordx2 s[40:41], s[0:1], s52\n"
    "s_cmp_eq_u32 s89, 2\n"
    "s_cselect_b32 s54, 72, s52\n"
    "s_load_dwordx2 s[36:37], s[0:1], s53\n"
    "s_load_dwordx2 s[44:45], s[0:1], s54\n"
    "s_load_dwordx2 s[8:9], s[0:1], 8\n"
    "s_load_dwordx2 s[12:13], s[0:1], 40\n"
    "s_load_dwordx2 s[16:17], s[0:1], 48\n"
    "s_load_dwordx2 s[20:21], s[0:1], 16\n"
    "s_load_dwordx2 s[24:25], s[0:1], 24\n"
    "s_load_dwordx2 s[28:29], s[0:1], 32\n"
    "s_add_u32 s92, s91, 64\n"
    "s_cmpk_eq_u32 s92, 448\n"
    "s_cselect_b32 s92, 0, s92\n"
    "s_add_u32 m0, s78, 20480\n"
    "s_lshl1_add_u32 s83, s92, s84\n"
    "buffer_load_dwordx4 v134, s[4:7], s83 offen lds\n"
    "buffer_load_dwordx4 v135, s[4:7], s83 offen offset:1024 lds\n"
    "s_mul_i32 s42, s73, 448\n"
    "s_mov_b32 s43, s7\n"
    "s_add_u32 s46, s42, 1024\n"
    "s_mov_b32 s47, s7\n"
    "s_mul_i32 s38, s77, 448\n"
    "s_mov_b32 s39, s7\n"
    "s_mov_b32 s10, 16777216\n"
    "s_mov_b32 s11, s7\n"
    "s_xor_b32 s57, s75, 1\n"
    "s_mul_i32 s97, s57, 14336\n"
    "v_bfe_u32 v131, v0, 2, 2\n"
    "v_and_b32 v132, 3, v0\n"
    "v_and_b32 v133, 15, v0\n"
    "v_lshrrev_b32 v140, 5, v0\n"
    "v_and_b32 v141, 240, v1\n"
    "v_lshrrev_b32 v143, 1, v133\n"
    "v_xor_b32 v142, v130, v143\n"
    "v_lshlrev_b32 v142, 4, v142\n"
    "v_lshl_add_u32 v142, v133, 7, v142\n"
    "v_add_u32 v138, s78, v142\n"
    "v_xor_b32 v139, 64, v138\n"
    "v_lshlrev_b32 v142, 1, v130\n"
    "v_xor_b32 v142, v142, v143\n"
    "v_and_b32 v143, 16, v1\n"
    "s_movk_i32 s81, 16256\n"
    "v_cmp_eq_u32 vcc, 0, v142\n"
    "v_lshlrev_b32 v143, v143, s81\n"
    "v_mov_b32 v148, 0x3f803f80\n"
    "v_mov_b32 v149, 0x3f803f80\n"
    "v_mov_b32 v150, 0x3f803f80\n"
    "s_add_u32 s93, s92, 64\n"
    "s_cmpk_eq_u32 s93, 448\n"
    "s_cselect_b32 s93, 0, s93\n"
    "s_add_u32 m0, s78, 24576\n"
    "s_lshl1_add_u32 s83, s93, s84\n"
    "buffer_load_dwordx4 v134, s[4:7], s83 offen lds\n"
    "buffer_load_dwordx4 v135, s[4:7], s83 offen offset:1024 lds\n"
    "v_mov_b32 v151, 0x3f803f80\n"
    "v_cndmask_b32 v154, 0, v143, vcc\n"
    "v_cmp_eq_u32 vcc, 1, v142\n"
    "v_mov_b32 v152, 0\n"
    "v_mov_b32 v153, 0\n"
    "v_mov_b32 v156, 0\n"
    "v_mov_b32 v157, 0\n"
    "v_cndmask_b32 v155, 0, v143, vcc\n"
    "s_lshr_b32 s74, s72, 2\n"
    "s_mul_i32 s81, s74, 7\n"
    "s_lshl1_add_u32 s76, s76, 1\n"
    "s_min_u32 s76, s76, 6\n"
    "s_add_u32 s76, s76, s81\n"
    "s_lshl_b32 s82, s76, 7\n"
    "s_sub_u32 s79, s82, 128\n"
    "s_lshl_b32 s81, s74, 8\n"
    "s_cmp_eq_u32 s89, 0\n"
    "s_cselect_b32 s79, s79, s81\n"
    "v_and_b32 v142, 2, v128\n"
    "v_xor_b32 v144, v129, v142\n"
    "v_lshlrev_b32 v144, 4, v144\n"
    "v_and_b32 v146, 1, v130\n"
    "v_lshl_add_u32 v158, s68, 4, v128\n"
    "v_mad_u32_u24 v142, v158, s77, v144\n"
    "v_add_u32 v10, s79, v142\n"
    "s_lshl3_add_u32 s81, s77, -1024\n"
    "v_xor_b32 v142, 64, v10\n"
    "v_add_u32 v11, s81, v142\n"
    "v_lshl_add_u32 v142, v140, 2, v131\n"
    "v_lshl_add_u32 v142, v146, 3, v142\n"
    "s_add_u32 s94, s93, 64\n"
    "s_cmpk_eq_u32 s94, 448\n"
    "s_cselect_b32 s94, 0, s94\n"
    "s_add_u32 m0, s78, 28672\n"
    "s_lshl1_add_u32 s83, s94, s84\n"
    "buffer_load_dwordx4 v134, s[4:7], s83 offen lds\n"
    "buffer_load_dwordx4 v135, s[4:7], s83 offen offset:1024 lds\n"
    "v_lshl_add_u32 v142, s68, 4, v142\n"
    "v_mul_u32_u24 v142, 56, v142\n"
    "v_lshlrev_b32 v143, 1, v146\n"
    "v_xor_b32 v143, v132, v143\n"
    "v_lshl_add_u32 v142, v143, 4, v142\n"
    "v_add_u32 v143, s80, v10\n"
    "v_cndmask_b32 v12, v143, v142, s[98:99]\n"
    "s_add_u32 s80, s80, 1024\n"
    "v_add_u32 v143, s80, v11\n"
    "v_cndmask_b32 v13, v143, v142, s[98:99]\n"
    "v_lshrrev_b32 v142, 1, v131\n"
    "v_lshl_add_u32 v142, v146, 1, v142\n"
    "v_mul_u32_u24 v147, 40960, v140\n"
    "v_lshl_add_u32 v147, v132, 3, v147\n"
    "v_lshl_add_u32 v143, v146, 10, v147\n"
    "v_lshl_add_u32 v143, v131, 7, v143\n"
    "v_lshl_add_u32 v2, v142, 5, v143\n"
    "v_mul_u32_u24 v142, 288, v146\n"
    "v_add_u32 v147, v147, v142\n"
    "v_lshl_add_u32 v147, v131, 6, v147\n"
    "v_xor_b32 v3, 32, v2\n"
    "v_xor_b32 v4, 64, v2\n"
    "v_xor_b32 v5, 96, v2\n"
    "v_cndmask_b32 v192, v2, v147, s[98:99]\n"
    "v_add_u32 v147, 1024, v147\n"
    "v_cndmask_b32 v194, v4, v147, s[98:99]\n"
    "v_xor_b32 v193, 32, v192\n"
    "v_xor_b32 v195, 32, v194\n"
    "v_add_u32 v196, 81920, v192\n"
    "v_add_u32 v197, 81920, v193\n"
    "s_add_u32 s95, s94, 64\n"
    "s_cmpk_eq_u32 s95, 448\n"
    "s_cselect_b32 s95, 0, s95\n"
    "s_add_u32 s96, s95, 64\n"
    "s_cmpk_eq_u32 s96, 448\n"
    "s_cselect_b32 s96, 0, s96\n"
    "; instruction prefetch stops at 4 KiB pages: visit the first line of each page once (branches only, no lane enabled)\n"
    "s_mov_b64 exec, 0\n"
    "s_cmp_eq_u32 s68, 0\n"
    "s_cbranch_scc1 .Lpage_1_%=\n"
    "s_cmp_eq_u32 s68, 1\n"
    "s_cbranch_scc1 .Lpage_2_%=\n"
    "s_cmp_eq_u32 s68, 2\n"
    "s_cbranch_scc1 .Lpage_3_%=\n"
    "s_branch .Lpage_4_%=\n"
    ".Lpage_0_%=:\n"
    "s_mov_b64 exec, -1\n"
    "s_waitcnt lgkmcnt(0)\n"
    "s_sub_u32 s44, s44, 1024\n"
    "s_subb_u32 s45, s45, 0\n"
    "; ---- x -> LDS -> registers; row sums and squares on the matrix pipe; x - mean likewise; rstd goes into the epilogues\n"
    "s_waitcnt vmcnt(8)\n"
    "ds_read_b128 v[16:19], v138 offset:12288\n"
    "ds_read_b128 v[20:23], v139 offset:12288\n"
    "s_add_u32 m0, s78, 32768\n"
    "s_lshl1_add_u32 s83, s95, s84\n"
    "buffer_load_dwordx4 v134, s[4:7], s83 offen lds\n"
    "buffer_load_dwordx4 v135, s[4:7], s83 offen offset:1024 lds\n"
    "v_add_u32 v198, 81920, v194\n"
    "v_add_u32 v199, 81920, v195\n"
    "v_add_u32 v6, 81920, v2\n"
    "v_add_u32 v7, 81920, v3\n"
    "v_add_u32 v8, 81920, v4\n"
    "v_add_u32 v9, 81920, v5\n"
    "v_mul_u32_u24 v142, 3584, v158\n"
    "v_add3_u32 v14, s82, v142, v144\n"
    "v_xor_b32 v142, 64, v14\n"
    "v_add_u32 v15, 27648, v142\n"
    "s_lshl_b32 s66, s2, 7\n"
    "s_waitcnt vmcnt(8)\n"
    "ds_read_b128 v[32:35], v138 offset:16384\n"
    "ds_read_b128 v[36:39], v139 offset:16384\n"
    "s_add_u32 m0, s78, 36864\n"
    "s_lshl1_add_u32 s83, s96, s84\n"
    "buffer_load_dwordx4 v134, s[4:7], s83 offen lds\n"
    "buffer_load_dwordx4 v135, s[4:7], s83 offen offset:1024 lds\n"
    "s_add_u32 s66, s66, 393216\n"
    "v_mov_b32 v216, 0x80000000\n"
    "v_mov_b32 v142, 0x80000000\n"
    "s_cmp_eq_u32 s68, 0\n"
    "s_cselect_b32 s79, 0, 0x80000000\n"
    "s_cselect_b32 s58, 1, 0\n"
    "v_writelane_b32 v216, s79, 0\n"
    "s_cmp_eq_u32 s89, 0\n"
    "s_cselect_b32 s79, 0x80000000, 0\n"
    "s_cselect_b32 s80, 0, 0x80000000\n"
    "v_or_b32 v217, s79, v216\n"
    "s_waitcnt lgkmcnt(2)\n"
    "v_mfma_f32_16x16x32_bf16 v[224:227], v[148:151], v[16:19], 0\n"
    "s_waitcnt vmcnt(8)\n"
    "ds_read_b128 v[48:51], v138 offset:20480\n"
    "v_mfma_f32_16x16x32_bf16 v[232:235], v[16:19], v[16:19], 0\n"
    "ds_read_b128 v[52:55], v139 offset:20480\n"
    "s_add_u32 m0, s78, 14336\n"
    "v_mfma_f32_16x16x32_bf16 v[224:227], v[148:151], v[20:23], v[224:227]\n"
    "s_lshl1_add_u32 s83, s90, s97\n"
    "buffer_load_dwordx4 v134, s[4:7], s83 offen lds\n"
    "v_mfma_f32_16x16x32_bf16 v[232:235], v[20:23], v[20:23], v[232:235]\n"
    "buffer_load_dwordx4 v135, s[4:7], s83 offen offset:1024 lds\n"
    "v_or_b32 v217, 32768, v217\n"
    "v_or_b32 v253, s80, v1\n"
    "v_cndmask_b32 v247, v142, v216, s[98:99]\n"
    "v_or_b32 v247, 65536, v247\n"
    "s_mul_i32 vcc_lo, s58, 15\n"
    "s_mov_b32 vcc_hi, 0\n"
    "v_lshlrev_b32 v143, 15, v0\n"
    "v_cndmask_b32 v245, v142, v143, vcc\n"
    "v_mov_b32 v219, 1\n"
    "v_mov_b32 v201, 1\n"
    "s_waitcnt lgkmcnt(2)\n"
    "v_mfma_f32_16x16x32_bf16 v[224:227], v[148:151], v[32:35], v[224:227]\n"
    "s_waitcnt vmcnt(8)\n"
    "ds_read_b128 v[64:67], v138 offset:24576\n"
    "v_mfma_f32_16x16x32_bf16 v[232:235], v[32:35], v[32:35], v[232:235]\n"
    "ds_read_b128 v[68:71], v139 offset:24576\n"
    "s_add_u32 m0, s78, 18432\n"
    "v_mfma_f32_16x16x32_bf16 v[224:227], v[148:151], v[36:39], v[224:227]\n"
    "s_lshl1_add_u32 s83, s91, s97\n"
    "buffer_load_dwordx4 v134, s[4:7], s83 offen lds\n"
    "v_mfma_f32_16x16x32_bf16 v[232:235], v[36:39], v[36:39], v[232:235]\n"
    "buffer_load_dwordx4 v135, s[4:7], s83 offen offset:1024 lds\n"
    "s_or_b32 s80, s2, s68\n"
    "s_cmp_eq_u32 s80, 0\n"
    "s_cselect_b32 s67, 255, 0\n"
    "s_cselect_b64 vcc, 255, 0\n"
    "v_lshlrev_b32 v143, 7, v0\n"
    "v_cndmask_b32 v218, v142, v143, vcc\n"
    "v_mov_b32 v203, 0x100000\n"
    "v_cmp_eq_u32 vcc, 0, v133\n"
    "v_lshlrev_b32 v143, 2, v130\n"
    "v_lshlrev_b32 v252, 6, v131\n"
    "s_waitcnt lgkmcnt(2)\n"
    "v_mfma_f32_16x16x32_bf16 v[224:227], v[148:151], v[48:51], v[224:227]\n"
    "s_waitcnt vmcnt(8)\n"
    "ds_read_b128 v[80:83], v138 offset:28672\n"
    "v_mfma_f32_16x16x32_bf16 v[232:235], v[48:51], v[48:51], v[232:235]\n"
    "ds_read_b128 v[84:87], v139 offset:28672\n"
    "s_add_u32 m0, s78, 22528\n"
    "v_mfma_f32_16x16x32_bf16 v[224:227], v[148:151], v[52:55], v[224:227]\n"
    "s_lshl1_add_u32 s83, s92, s97\n"
    "buffer_load_dwordx4 v134, s[4:7], s83 offen lds\n"
    "v_mfma_f32_16x16x32_bf16 v[232:235], v[52:55], v[52:55], v[232:235]\n"
    "buffer_load_dwordx4 v135, s[4:7], s83 offen offset:1024 lds\n"
    "v_cndmask_b32 v143, v142, v143, vcc\n"
    "v_lshl_add_u32 v252, v133, 2, v252\n"
    "v_cndmask_b32 v244, v142, v143, s[98:99]\n"
    "v_lshl_add_u32 v240, v130, 10, v141\n"
    "v_or_b32 v240, s79, v240\n"
    "v_lshl_add_u32 v142, v130, 9, v141\n"
    "v_cndmask_b32 v241, v240, v142, s[98:99]\n"
    "v_add_u32 v142, 8192, v240\n"
    "v_add_u32 v143, 2048, v1\n"
    "v_cndmask_b32 v254, v142, v143, s[98:99]\n"
    "s_waitcnt lgkmcnt(2)\n"
    "v_mfma_f32_16x16x32_bf16 v[224:227], v[148:151], v[64:67], v[224:227]\n"
    "s_waitcnt vmcnt(8)\n"
    "ds_read_b128 v[96:99], v138 offset:32768\n"
    "v_mfma_f32_16x16x32_bf16 v[232:235], v[64:67], v[64:67], v[232:235]\n"
    "ds_read_b128 v[100:103], v139 offset:32768\n"
    "s_add_u32 m0, s78, 26624\n"
    "v_mfma_f32_16x16x32_bf16 v[224:227], v[148:151], v[68:71], v[224:227]\n"
    "s_lshl1_add_u32 s83, s93, s97\n"
    "buffer_load_dwordx4 v134, s[4:7], s83 offen lds\n"
    "v_mfma_f32_16x16x32_bf16 v[232:235], v[68:71], v[68:71], v[232:235]\n"
    "buffer_load_dwordx4 v135, s[4:7], s83 offen offset:1024 lds\n"
    "v_mul_u32_u24 v242, 72, v133\n"
    "v_lshl_add_u32 v242, v130, 3, v242\n"
    "v_add_u32 v242, s78, v242\n"
    "v_lshl_add_u32 v243, v140, 2, v131\n"
    "v_mul_u32_u24 v243, 72, v243\n"
    "v_lshl_add_u32 v243, v146, 5, v243\n"
    "v_lshl_add_u32 v243, v132, 3, v243\n"
    "v_add_u32 v243, s78, v243\n"
    "v_and_b32 v142, 6, v128\n"
    "v_xor_b32 v142, v142, v129\n"
    "s_waitcnt lgkmcnt(2)\n"
    "v_mfma_f32_16x16x32_bf16 v[224:227], v[148:151], v[80:83], v[224:227]\n"
    "s_waitcnt vmcnt(8)\n"
    "ds_read_b128 v[112:115], v138 offset:36864\n"
    "v_mfma_f32_16x16x32_bf16 v[232:235], v[80:83], v[80:83], v[232:235]\n"
    "ds_read_b128 v[116:119], v139 offset:36864\n"
    "s_add_u32 m0, s78, 30720\n"
    "v_mfma_f32_16x16x32_bf16 v[224:227], v[148:151], v[84:87], v[224:227]\n"
    "s_lshl1_add_u32 s83, s94, s97\n"
    "buffer_load_dwordx4 v134, s[4:7], s83 offen lds\n"
    "v_mfma_f32_16x16x32_bf16 v[232:235], v[84:87], v[84:87], v[232:235]\n"
    "buffer_load_dwordx4 v135, s[4:7], s83 offen offset:1024 lds\n"
    "v_lshl_add_u32 v142, v142, 4, v145\n"
    "v_lshl_add_u32 v142, s88, 7, v142\n"
    "s_mul_i32 s56, s68, 401408\n"
    "v_add_u32 v204, s56, v142\n"
    "v_add_u32 v205, 6144, v204\n"
    "v_add_u32 v206, 12288, v204\n"
    "v_add_u32 v207, 18432, v204\n"
    "v_lshrrev_b32 v142, 2, v0\n"
    "v_lshlrev_b32 v143, 3, v132\n"
    "v_lshl_add_u32 v142, v142, 7, v143\n"
    "s_waitcnt lgkmcnt(2)\n"
    "v_mfma_f32_16x16x32_bf16 v[224:227], v[148:151], v[96:99], v[224:227]\n"
    "s_waitcnt vmcnt(8)\n"
    "ds_read_b128 v[24:27], v138 offset:14336\n"
    "v_mfma_f32_16x16x32_bf16 v[232:235], v[96:99], v[96:99], v[232:235]\n"
    "ds_read_b128 v[28:31], v139 offset:14336\n"
    "s_add_u32 m0, s78, 34816\n"
    "v_mfma_f32_16x16x32_bf16 v[224:227], v[148:151], v[100:103], v[224:227]\n"
    "s_lshl1_add_u32 s83, s95, s97\n"
    "buffer_load_dwordx4 v134, s[4:7], s83 offen lds\n"
    "v_mfma_f32_16x16x32_bf16 v[232:235], v[100:103], v[100:103], v[232:235]\n"
    "buffer_load_dwordx4 v135, s[4:7], s83 offen offset:1024 lds\n"
    "v_and_b32 v143, 3, v128\n"
    "v_lshl_add_u32 v142, v143, 5, v142\n"
    "v_add_u32 v208, s78, v142\n"
    "v_lshl_add_u32 v222, v140, 9, v1\n"
    "v_lshl_add_u32 v223, v146, 15, v141\n"
    "v_lshl_add_u32 v223, v140, 8, v223\n"
    "v_lshl_add_u32 v221, v132, 3, v131\n"
    "v_lshlrev_b32 v221, 7, v221\n"
    "s_and_b32 s82, s70, 1\n"
    "s_lshl1_add_u32 s85, s69, s82\n"
    "s_waitcnt lgkmcnt(2)\n"
    "v_mfma_f32_16x16x32_bf16 v[224:227], v[148:151], v[112:115], v[224:227]\n"
    "s_waitcnt vmcnt(8)\n"
    "ds_read_b128 v[40:43], v138 offset:18432\n"
    "v_mfma_f32_16x16x32_bf16 v[232:235], v[112:115], v[112:115], v[232:235]\n"
    "ds_read_b128 v[44:47], v139 offset:18432\n"
    "s_add_u32 m0, s78, 38912\n"
    "v_mfma_f32_16x16x32_bf16 v[224:227], v[148:151], v[116:119], v[224:227]\n"
    "s_lshl1_add_u32 s83, s96, s97\n"
    "buffer_load_dwordx4 v134, s[4:7], s83 offen lds\n"
    "v_mfma_f32_16x16x32_bf16 v[232:235], v[116:119], v[116:119], v[232:235]\n"
    "buffer_load_dwordx4 v135, s[4:7], s83 offen offset:1024 lds\n"
    "s_lshr_b32 s86, s70, 1\n"
    "s_mov_b32 s18, 1605632\n"
    "s_mov_b32 s19, s7\n"
    "s_mov_b64 s[14:15], s[18:19]\n"
    "s_mov_b64 s[22:23], s[18:19]\n"
    "s_mov_b32 s26, 401408\n"
    "s_mov_b32 s27, s7\n"
    "s_mov_b64 s[30:31], s[6:7]\n"
    "s_mov_b64 s[32:33], s[8:9]\n"
    "s_mov_b64 s[34:35], s[10:11]\n"
    "s_waitcnt lgkmcnt(2)\n"
    "v_mfma_f32_16x16x32_bf16 v[228:231], v[148:151], v[24:27], 0\n"
    "s_waitcnt vmcnt(8)\n"
    "ds_read_b128 v[56:59], v138 offset:22528\n"
    "ds_read_b128 v[60:63], v139 offset:22528\n"
    "v_mfma_f32_16x16x32_bf16 v[236:239], v[24:27], v[24:27], 0\n"
    "s_add_u32 m0, s78, 0\n"
    "s_mul_i32 s83, s90, s73\n"
    "buffer_load_dwordx4 v12, s[40:43], s83 offen lds\n"
    "v_mfma_f32_16x16x32_bf16 v[228:231], v[148:151], v[28:31], v[228:231]\n"
    "buffer_load_dwordx4 v13, s[44:47], s83 offen offset:1024 lds\n"
    "v_mul_f32 v224, 0xbb124925, v224\n"
    "v_mul_f32 v225, 0xbb124925, v225\n"
    "v_mfma_f32_16x16x32_bf16 v[236:239], v[28:31], v[28:31], v[236:239]\n"
    "v_mul_f32 v226, 0xbb124925, v226\n"
    "v_mul_f32 v227, 0xbb124925, v227\n"
    "s_nop 1\n"
    "v_mfma_f32_16x16x32_bf16 v[160:163], v[154:157], v[16:19], v[224:227]\n"
    "v_mfma_f32_16x16x32_bf16 v[164:167], v[152:155], v[16:19], v[224:227]\n"
    "v_mfma_f32_16x16x32_bf16 v[168:171], v[154:157], v[20:23], v[224:227]\n"
    "v_mfma_f32_16x16x32_bf16 v[172:175], v[152:155], v[20:23], v[224:227]\n"
    "s_waitcnt lgkmcnt(2)\n"
    "v_mfma_f32_16x16x32_bf16 v[228:231], v[148:151], v[40:43], v[228:231]\n"
    "s_waitcnt vmcnt(8)\n"
    "ds_read_b128 v[72:75], v138 offset:26624\n"
    "v_mfma_f32_16x16x32_bf16 v[236:239], v[40:43], v[40:43], v[236:239]\n"
    "ds_read_b128 v[76:79], v139 offset:26624\n"
    "s_add_u32 m0, s78, 2048\n"
    "v_mfma_f32_16x16x32_bf16 v[228:231], v[148:151], v[44:47], v[228:231]\n"
    "s_mul_i32 s83, s91, s73\n"
    "buffer_load_dwordx4 v12, s[40:43], s83 offen lds\n"
    "v_mfma_f32_16x16x32_bf16 v[236:239], v[44:47], v[44:47], v[236:239]\n"
    "buffer_load_dwordx4 v13, s[44:47], s83 offen offset:1024 lds\n"
    "v_mfma_f32_16x16x32_bf16 v[176:179], v[154:157], v[32:35], v[224:227]\n"
    "v_cvt_pk_bf16_f32 v16, v160, v161\n"
    "v_cvt_pk_bf16_f32 v17, v162, v163\n"
    "v_mfma_f32_16x16x32_bf16 v[180:183], v[152:155], v[32:35], v[224:227]\n"
    "v_cvt_pk_bf16_f32 v18, v164, v165\n"
    "v_cvt_pk_bf16_f32 v19, v166, v167\n"
    "v_mfma_f32_16x16x32_bf16 v[184:187], v[154:157], v[36:39], v[224:227]\n"
    "v_cvt_pk_bf16_f32 v20, v168, v169\n"
    "v_cvt_pk_bf16_f32 v21, v170, v171\n"
    "v_mfma_f32_16x16x32_bf16 v[188:191], v[152:155], v[36:39], v[224:227]\n"
    "v_cvt_pk_bf16_f32 v22, v172, v173\n"
    "v_cvt_pk_bf16_f32 v23, v174, v175\n"
    "s_waitcnt lgkmcnt(2)\n"
    "v_mfma_f32_16x16x32_bf16 v[228:231], v[148:151], v[56:59], v[228:231]\n"
    "s_waitcnt vmcnt(8)\n"
    "ds_read_b128 v[88:91], v138 offset:30720\n"
    "v_mfma_f32_16x16x32_bf16 v[236:239], v[56:59], v[56:59], v[236:239]\n"
    "ds_read_b128 v[92:95], v139 offset:30720\n"
    "s_add_u32 m0, s78, 4096\n"
    "v_mfma_f32_16x16x32_bf16 v[228:231], v[148:151], v[60:63], v[228:231]\n"
    "s_mul_i32 s83, s92, s73\n"
    "buffer_load_dwordx4 v12, s[40:43], s83 offen lds\n"
    "v_mfma_f32_16x16x32_bf16 v[236:239], v[60:63], v[60:63], v[236:239]\n"
    "buffer_load_dwordx4 v13, s[44:47], s83 offen offset:1024 lds\n"
    "v_mfma_f32_16x16x32_bf16 v[160:163], v[154:157], v[48:51], v[224:227]\n"
    "v_cvt_pk_bf16_f32 v32, v176, v177\n"
    "v_cvt_pk_bf16_f32 v33, v178, v179\n"
    "v_mfma_f32_16x16x32_bf16 v[164:167], v[152:155], v[48:51], v[224:227]\n"
    "v_cvt_pk_bf16_f32 v34, v180, v181\n"
    "v_cvt_pk_bf16_f32 v35, v182, v183\n"
    "v_mfma_f32_16x16x32_bf16 v[168:171], v[154:157], v[52:55], v[224:227]\n"
    "v_cvt_pk_bf16_f32 v36, v184, v185\n"
    "v_cvt_pk_bf16_f32 v37, v186, v187\n"
    "v_mfma_f32_16x16x32_bf16 v[172:175], v[152:155], v[52:55], v[224:227]\n"
    "v_cvt_pk_bf16_f32 v38, v188, v189\n"
    "v_cvt_pk_bf16_f32 v39, v190, v191\n"
    "s_waitcnt lgkmcnt(2)\n"
    "v_mfma_f32_16x16x32_bf16 v[228:231], v[148:151], v[72:75], v[228:231]\n"
    "s_waitcnt vmcnt(8)\n"
    "ds_read_b128 v[104:107], v138 offset:34816\n"
    "v_mfma_f32_16x16x32_bf16 v[236:239], v[72:75], v[72:75], v[236:239]\n"
    "ds_read_b128 v[108:111], v139 offset:34816\n"
    "s_add_u32 m0, s78, 6144\n"
    "v_mfma_f32_16x16x32_bf16 v[228:231], v[148:151], v[76:79], v[228:231]\n"
    "s_mul_i32 s83, s93, s73\n"
    "buffer_load_dwordx4 v12, s[40:43], s83 offen lds\n"
    "v_mfma_f32_16x16x32_bf16 v[236:239], v[76:79], v[76:79], v[236:239]\n"
    "buffer_load_dwordx4 v13, s[44:47], s83 offen offset:1024 lds\n"
    "v_mfma_f32_16x16x32_bf16 v[176:179], v[154:157], v[64:67], v[224:227]\n"
    "v_cvt_pk_bf16_f32 v48, v160, v161\n"
    "v_cvt_pk_bf16_f32 v49, v162, v163\n"
    "v_mfma_f32_16x16x32_bf16 v[180:183], v[152:155], v[64:67], v[224:227]\n"
    "v_cvt_pk_bf16_f32 v50, v164, v165\n"
    "v_cvt_pk_bf16_f32 v51, v166, v167\n"
    "v_mfma_f32_16x16x32_bf16 v[184:187], v[154:157], v[68:71], v[224:227]\n"
    "v_cvt_pk_bf16_f32 v52, v168, v169\n"
    "v_cvt_pk_bf16_f32 v53, v170, v171\n"
    "v_mfma_f32_16x16x32_bf16 v[188:191], v[152:155], v[68:71], v[224:227]\n"
    "v_cvt_pk_bf16_f32 v54, v172, v173\n"
    "v_cvt_pk_bf16_f32 v55, v174, v175\n"
    "buffer_atomic_add v203, v218, s[8:11], 0 offen sc0\n"
    "buffer_store_dword v152, v245, s[8:11], s66 offen sc1\n"
    "s_waitcnt lgkmcnt(2)\n"
    "v_mfma_f32_16x16x32_bf16 v[228:231], v[148:151], v[88:91], v[228:231]\n"
    "s_waitcnt vmcnt(10)\n"
    "ds_read_b128 v[120:123], v138 offset:38912\n"
    "v_mfma_f32_16x16x32_bf16 v[236:239], v[88:91], v[88:91], v[236:239]\n"
    "ds_read_b128 v[124:127], v139 offset:38912\n"
    "s_add_u32 m0, s78, 8192\n"
    "v_mfma_f32_16x16x32_bf16 v[228:231], v[148:151], v[92:95], v[228:231]\n"
    "s_mul_i32 s83, s94, s73\n"
    "buffer_load_dwordx4 v12, s[40:43], s83 offen lds\n"
    "v_mfma_f32_16x16x32_bf16 v[236:239], v[92:95], v[92:95], v[236:239]\n"
    "buffer_load_dwordx4 v13, s[44:47], s83 offen offset:1024 lds\n"
    "v_mfma_f32_16x16x32_bf16 v[160:163], v[154:157], v[80:83], v[224:227]\n"
    "v_cvt_pk_bf16_f32 v64, v176, v177\n"
    "v_cvt_pk_bf16_f32 v65, v178, v179\n"
    "v_mfma_f32_16x16x32_bf16 v[164:167], v[152:155], v[80:83], v[224:227]\n"
    "v_cvt_pk_bf16_f32 v66, v180, v181\n"
    "v_cvt_pk_bf16_f32 v67, v182, v183\n"
    "v_mfma_f32_16x16x32_bf16 v[168:171], v[154:157], v[84:87], v[224:227]\n"
    "v_cvt_pk_bf16_f32 v68, v184, v185\n"
    "v_cvt_pk_bf16_f32 v69, v186, v187\n"
    "v_mfma_f32_16x16x32_bf16 v[172:175], v[152:155], v[84:87], v[224:227]\n"
    "v_cvt_pk_bf16_f32 v70, v188, v189\n"
    "v_cvt_pk_bf16_f32 v71, v190, v191\n"
    "s_waitcnt lgkmcnt(2)\n"
    "v_mfma_f32_16x16x32_bf16 v[228:231], v[148:151], v[104:107], v[228:231]\n"
    "v_cvt_pk_bf16_f32 v80, v160, v161\n"
    "v_cvt_pk_bf16_f32 v81, v162, v163\n"
    "v_mfma_f32_16x16x32_bf16 v[236:239], v[104:107], v[104:107], v[236:239]\n"
    "v_cvt_pk_bf16_f32 v82, v164, v165\n"
    "v_mfma_f32_16x16x32_bf16 v[228:231], v[148:151], v[108:111], v[228:231]\n"
    "v_cvt_pk_bf16_f32 v83, v166, v167\n"
    "v_mfma_f32_16x16x32_bf16 v[236:239], v[108:111], v[108:111], v[236:239]\n"
    "s_waitcnt lgkmcnt(0)\n"
    "v_mfma_f32_16x16x32_bf16 v[176:179], v[154:157], v[96:99], v[224:227]\n"
    "v_cvt_pk_bf16_f32 v84, v168, v169\n"
    "v_mfma_f32_16x16x32_bf16 v[180:183], v[152:155], v[96:99], v[224:227]\n"
    "v_cvt_pk_bf16_f32 v85, v170, v171\n"
    "v_mfma_f32_16x16x32_bf16 v[184:187], v[154:157], v[100:103], v[224:227]\n"
    "v_cvt_pk_bf16_f32 v86, v172, v173\n"
    "v_mfma_f32_16x16x32_bf16 v[188:191], v[152:155], v[100:103], v[224:227]\n"
    "v_cvt_pk_bf16_f32 v87, v174, v175\n"
    "v_mfma_f32_16x16x32_bf16 v[228:231], v[148:151], v[120:123], v[228:231]\n"
    "s_add_u32 m0, s78, 10240\n"
    "s_mul_i32 s83, s95, s73\n"
    "v_mfma_f32_16x16x32_bf16 v[236:239], v[120:123], v[120:123], v[236:239]\n"
    "buffer_load_dwordx4 v12, s[40:43], s83 offen lds\n"
    "v_mfma_f32_16x16x32_bf16 v[228:231], v[148:151], v[124:127], v[228:231]\n"
    "buffer_load_dwordx4 v13, s[44:47], s83 offen offset:1024 lds\n"
    "v_mfma_f32_16x16x32_bf16 v[236:239], v[124:127], v[124:127], v[236:239]\n"
    "v_mfma_f32_16x16x32_bf16 v[160:163], v[154:157], v[112:115], v[224:227]\n"
    "v_cvt_pk_bf16_f32 v96, v176, v177\n"
    "v_cvt_pk_bf16_f32 v97, v178, v179\n"
    "v_mfma_f32_16x16x32_bf16 v[164:167], v[152:155], v[112:115], v[224:227]\n"
    "v_cvt_pk_bf16_f32 v98, v180, v181\n"
    "v_cvt_pk_bf16_f32 v99, v182, v183\n"
    "v_mfma_f32_16x16x32_bf16 v[168:171], v[154:157], v[116:119], v[224:227]\n"
    "v_cvt_pk_bf16_f32 v100, v184, v185\n"
    "v_cvt_pk_bf16_f32 v101, v186, v187\n"
    "v_mfma_f32_16x16x32_bf16 v[172:175], v[152:155], v[116:119], v[224:227]\n"
    "v_cvt_pk_bf16_f32 v102, v188, v189\n"
    "v_cvt_pk_bf16_f32 v103, v190, v191\n"
    "v_mul_f32 v228, 0xbb124925, v228\n"
    "v_mul_f32 v229, 0xbb124925, v229\n"
    "v_mul_f32 v230, 0xbb124925, v230\n"
    "v_mul_f32 v231, 0xbb124925, v231\n"
    "s_nop 1\n"
    "v_mfma_f32_16x16x32_bf16 v[176:179], v[154:157], v[24:27], v[228:231]\n"
    "v_cvt_pk_bf16_f32 v112, v160, v161\n"
    "v_cvt_pk_bf16_f32 v113, v162, v163\n"
    "v_mfma_f32_16x16x32_bf16 v[180:183], v[152:155], v[24:27], v[228:231]\n"
    "v_cvt_pk_bf16_f32 v114, v164, v165\n"
    "v_cvt_pk_bf16_f32 v115, v166, v167\n"
    "v_mfma_f32_16x16x32_bf16 v[184:187], v[154:157], v[28:31], v[228:231]\n"
    "v_cvt_pk_bf16_f32 v116, v168, v169\n"
    "v_cvt_pk_bf16_f32 v117, v170, v171\n"
    "v_mfma_f32_16x16x32_bf16 v[188:191], v[152:155], v[28:31], v[228:231]\n"
    "v_cvt_pk_bf16_f32 v118, v172, v173\n"
    "v_cvt_pk_bf16_f32 v119, v174, v175\n"
    "v_mfma_f32_16x16x32_bf16 v[160:163], v[154:157], v[40:43], v[228:231]\n"
    "s_add_u32 m0, s78, 12288\n"
    "s_mul_i32 s83, s96, s73\n"
    "buffer_load_dwordx4 v12, s[40:43], s83 offen lds\n"
    "v_mfma_f32_16x16x32_bf16 v[164:167], v[152:155], v[40:43], v[228:231]\n"
    "buffer_load_dwordx4 v13, s[44:47], s83 offen offset:1024 lds\n"
    "v_cvt_pk_bf16_f32 v24, v176, v177\n"
    "v_cvt_pk_bf16_f32 v25, v178, v179\n"
    "v_mfma_f32_16x16x32_bf16 v[168:171], v[154:157], v[44:47], v[228:231]\n"
    "v_cvt_pk_bf16_f32 v26, v180, v181\n"
    "v_cvt_pk_bf16_f32 v27, v182, v183\n"
    "v_cvt_pk_bf16_f32 v28, v184, v185\n"
    "v_mfma_f32_16x16x32_bf16 v[172:175], v[152:155], v[44:47], v[228:231]\n"
    "v_cvt_pk_bf16_f32 v29, v186, v187\n"
    "v_cvt_pk_bf16_f32 v30, v188, v189\n"
    "v_cvt_pk_bf16_f32 v31, v190, v191\n"
    "v_mfma_f32_16x16x32_bf16 v[176:179], v[154:157], v[56:59], v[228:231]\n"
    "v_cvt_pk_bf16_f32 v40, v160, v161\n"
    "v_cvt_pk_bf16_f32 v41, v162, v163\n"
    "v_mfma_f32_16x16x32_bf16 v[180:183], v[152:155], v[56:59], v[228:231]\n"
    "v_cvt_pk_bf16_f32 v42, v164, v165\n"
    "v_cvt_pk_bf16_f32 v43, v166, v167\n"
    "v_mfma_f32_16x16x32_bf16 v[184:187], v[154:157], v[60:63], v[228:231]\n"
    "v_cvt_pk_bf16_f32 v44, v168, v169\n"
    "v_cvt_pk_bf16_f32 v45, v170, v171\n"
    "v_mfma_f32_16x16x32_bf16 v[188:191], v[152:155], v[60:63], v[228:231]\n"
    "v_cvt_pk_bf16_f32 v46, v172, v173\n"
    "v_cvt_pk_bf16_f32 v47, v174, v175\n"
    "v_mfma_f32_16x16x32_bf16 v[160:163], v[154:157], v[72:75], v[228:231]\n"
    "s_add_u32 m0, s78, 14336\n"
    "s_mul_i32 s83, s90, s77\n"
    "buffer_load_dwordx4 v10, s[36:39], s83 offen lds\n"
    "v_mfma_f32_16x16x32_bf16 v[164:167], v[152:155], v[72:75], v[228:231]\n"
    "buffer_load_dwordx4 v11, s[36:39], s83 offen offset:1024 lds\n"
    "v_cvt_pk_bf16_f32 v56, v176, v177\n"
    "v_cvt_pk_bf16_f32 v57, v178, v179\n"
    "v_mfma_f32_16x16x32_bf16 v[168:171], v[154:157], v[76:79], v[228:231]\n"
    "v_cvt_pk_bf16_f32 v58, v180, v181\n"
    "v_cvt_pk_bf16_f32 v59, v182, v183\n"
    "v_cvt_pk_bf16_f32 v60, v184, v185\n"
    "v_mfma_f32_16x16x32_bf16 v[172:175], v[152:155], v[76:79], v[228:231]\n"
    "v_cvt_pk_bf16_f32 v61, v186, v187\n"
    "v_cvt_pk_bf16_f32 v62, v188, v189\n"
    "v_cvt_pk_bf16_f32 v63, v190, v191\n"
    "s_waitcnt vmcnt(14)\n"
    "s_barrier\n"
    "v_mfma_f32_16x16x32_bf16 v[176:179], v[154:157], v[88:91], v[228:231]\n"
    "ds_read_b64_tr_b16 a[0:1], v192\n"
    "v_cvt_pk_bf16_f32 v72, v160, v161\n"
    "v_cvt_pk_bf16_f32 v73, v162, v163\n"
    "v_mfma_f32_16x16x32_bf16 v[180:183], v[152:155], v[88:91], v[228:231]\n"
    "ds_read_b64_tr_b16 a[2:3], v192 offset:512\n"
    "v_cvt_pk_bf16_f32 v74, v164, v165\n"
    "v_cvt_pk_bf16_f32 v75, v166, v167\n"
    "v_mfma_f32_16x16x32_bf16 v[184:187], v[154:157], v[92:95], v[228:231]\n"
    "ds_read_b64_tr_b16 a[4:5], v196\n"
    "v_cvt_pk_bf16_f32 v76, v168, v169\n"
    ".Lpage_1_%=:\n"
    "s_cbranch_execz .Lpage_5_%=\n"
    "v_cvt_pk_bf16_f32 v77, v170, v171\n"
    "v_mfma_f32_16x16x32_bf16 v[188:191], v[152:155], v[92:95], v[228:231]\n"
    "ds_read_b64_tr_b16 a[6:7], v196 offset:512\n"
    "v_cvt_pk_bf16_f32 v78, v172, v173\n"
    "v_cvt_pk_bf16_f32 v79, v174, v175\n"
    "v_mfma_f32_16x16x32_bf16 v[160:163], v[154:157], v[104:107], v[228:231]\n"
    "ds_read_b64_tr_b16 a[8:9], v192 offset:2048\n"
    "v_cvt_pk_bf16_f32 v88, v176, v177\n"
    "v_cvt_pk_bf16_f32 v89, v178, v179\n"
    "v_mfma_f32_16x16x32_bf16 v[164:167], v[152:155], v[104:107], v[228:231]\n"
    "ds_read_b64_tr_b16 a[10:11], v192 offset:2560\n"
    "v_cvt_pk_bf16_f32 v90, v180, v181\n"
    "v_cvt_pk_bf16_f32 v91, v182, v183\n"
    "v_mfma_f32_16x16x32_bf16 v[168:171], v[154:157], v[108:111], v[228:231]\n"
    "ds_read_b64_tr_b16 a[12:13], v196 offset:2048\n"
    "v_cvt_pk_bf16_f32 v92, v184, v185\n"
    "v_cvt_pk_bf16_f32 v93, v186, v187\n"
    "v_mfma_f32_16x16x32_bf16 v[172:175], v[152:155], v[108:111], v[228:231]\n"
    "ds_read_b64_tr_b16 a[14:15], v196 offset:2560\n"
    "v_cvt_pk_bf16_f32 v94, v188, v189\n"
    "v_cvt_pk_bf16_f32 v95, v190, v191\n"
    "v_mfma_f32_16x16x32_bf16 v[176:179], v[154:157], v[120:123], v[228:231]\n"
    "ds_read_b64_tr_b16 a[16:17], v193\n"
    "v_cvt_pk_bf16_f32 v104, v160, v161\n"
    "v_cvt_pk_bf16_f32 v105, v162, v163\n"
    "v_mfma_f32_16x16x32_bf16 v[180:183], v[152:155], v[120:123], v[228:231]\n"
    "ds_read_b64_tr_b16 a[18:19], v193 offset:512\n"
    "v_cvt_pk_bf16_f32 v106, v164, v165\n"
    "v_cvt_pk_bf16_f32 v107, v166, v167\n"
    "v_mfma_f32_16x16x32_bf16 v[184:187], v[154:157], v[124:127], v[228:231]\n"
    "ds_read_b64_tr_b16 a[20:21], v197\n"
    "v_cvt_pk_bf16_f32 v108, v168, v169\n"
    "v_cvt_pk_bf16_f32 v109, v170, v171\n"
    "v_mfma_f32_16x16x32_bf16 v[188:191], v[152:155], v[124:127], v[228:231]\n"
    "ds_read_b64_tr_b16 a[22:23], v197 offset:512\n"
    "v_cvt_pk_bf16_f32 v110, v172, v173\n"
    "v_cvt_pk_bf16_f32 v111, v174, v175\n"
    "; ---- phase 1 main loop: 28 half-stages x 4 units x (2 k-steps x 2 token tiles)\n"
    "v_cvt_pk_bf16_f32 v120, v176, v177\n"
    "v_cvt_pk_bf16_f32 v121, v178, v179\n"
    "v_cvt_pk_bf16_f32 v122, v180, v181\n"
    "v_cvt_pk_bf16_f32 v123, v182, v183\n"
    "v_cvt_pk_bf16_f32 v124, v184, v185\n"
    "v_cvt_pk_bf16_f32 v125, v186, v187\n"
    "v_cvt_pk_bf16_f32 v126, v188, v189\n"
    "v_cvt_pk_bf16_f32 v127, v190, v191\n"
    "s_waitcnt lgkmcnt(4)\n"
    "v_mfma_f32_16x16x32_bf16 v[160:163], a[0:3], v[16:19], 0\n"
    "ds_read_b64_tr_b16 a[24:25], v193 offset:2048\n"
    "s_add_u32 m0, s78, 16384\n"
    "v_mfma_f32_16x16x32_bf16 v[160:163], a[4:7], v[20:23], v[160:163]\n"
    "ds_read_b64_tr_b16 a[26:27], v193 offset:2560\n"
    "s_mul_i32 s83, s91, s77\n"
    "v_mfma_f32_16x16x32_bf16 v[164:167], a[0:3], v[24:27], 0\n"
    "ds_read_b64_tr_b16 a[28:29], v197 offset:2048\n"
    "buffer_load_dwordx4 v10, s[36:39], s83 offen lds\n"
    "v_mfma_f32_16x16x32_bf16 v[164:167], a[4:7], v[28:31], v[164:167]\n"
    "ds_read_b64_tr_b16 a[30:31], v197 offset:2560\n"
    "v_and_b32 v246, 1, v0\n"
    "v_mfma_f32_16x16x32_bf16 v[164:167], a[8:11], v[40:43], v[164:167]\n"
    "ds_read_b64_tr_b16 a[32:33], v194\n"
    "v_cmp_eq_u32 vcc, 1, v246\n"
    "v_mfma_f32_16x16x32_bf16 v[164:167], a[12:15], v[44:47], v[164:167]\n"
    "ds_read_b64_tr_b16 a[34:35], v194 offset:512\n"
    "buffer_load_dwordx4 v11, s[36:39], s83 offen offset:1024 lds\n"
    "v_mfma_f32_16x16x32_bf16 v[160:163], a[8:11], v[32:35], v[160:163]\n"
    "ds_read_b64_tr_b16 a[36:37], v198\n"
    "v_and_b32 v246, 2, v0\n"
    "v_mfma_f32_16x16x32_bf16 v[160:163], a[12:15], v[36:39], v[160:163]\n"
    "ds_read_b64_tr_b16 a[38:39], v198 offset:512\n"
    "s_waitcnt lgkmcnt(4)\n"
    "v_mfma_f32_16x16x32_bf16 v[168:171], a[16:19], v[16:19], 0\n"
    "ds_read_b64_tr_b16 a[40:41], v194 offset:2048\n"
    "v_cmp_eq_u32 s[0:1], 2, v246\n"
    "v_mfma_f32_16x16x32_bf16 v[168:171], a[20:23], v[20:23], v[168:171]\n"
    "ds_read_b64_tr_b16 a[42:43], v194 offset:2560\n"
    "v_cndmask_b32 v232, v232, v233, vcc\n"
    "v_mfma_f32_16x16x32_bf16 v[172:175], a[16:19], v[24:27], 0\n"
    "ds_read_b64_tr_b16 a[44:45], v198 offset:2048\n"
    "v_cndmask_b32 v234, v234, v235, vcc\n"
    "v_mfma_f32_16x16x32_bf16 v[172:175], a[20:23], v[28:31], v[172:175]\n"
    "ds_read_b64_tr_b16 a[46:47], v198 offset:2560\n"
    "v_cndmask_b32 v232, v232, v234, s[0:1]\n"
    "v_mfma_f32_16x16x32_bf16 v[172:175], a[24:27], v[40:43], v[172:175]\n"
    "ds_read_b64_tr_b16 a[0:1], v195\n"
    "s_add_u32 m0, s78, 18432\n"
    "v_mfma_f32_16x16x32_bf16 v[172:175], a[28:31], v[44:47], v[172:175]\n"
    "ds_read_b64_tr_b16 a[2:3], v195 offset:512\n"
    "s_mul_i32 s83, s92, s77\n"
    "v_mfma_f32_16x16x32_bf16 v[168:171], a[24:27], v[32:35], v[168:171]\n"
    "ds_read_b64_tr_b16 a[4:5], v199\n"
    "buffer_load_dwordx4 v10, s[36:39], s83 offen lds\n"
    "v_mfma_f32_16x16x32_bf16 v[168:171], a[28:31], v[36:39], v[168:171]\n"
    "ds_read_b64_tr_b16 a[6:7], v199 offset:512\n"
    "s_waitcnt lgkmcnt(4)\n"
    "v_mfma_f32_16x16x32_bf16 v[176:179], a[32:35], v[16:19], 0\n"
    "ds_read_b64_tr_b16 a[8:9], v195 offset:2048\n"
    "ds_bpermute_b32 v233, v252, v232\n"
    "v_mfma_f32_16x16x32_bf16 v[176:179], a[36:39], v[20:23], v[176:179]\n"
    "ds_read_b64_tr_b16 a[10:11], v195 offset:2560\n"
    "buffer_load_dwordx4 v11, s[36:39], s83 offen offset:1024 lds\n"
    "v_mfma_f32_16x16x32_bf16 v[180:183], a[32:35], v[24:27], 0\n"
    "ds_read_b64_tr_b16 a[12:13], v199 offset:2048\n"
    "s_waitcnt vmcnt(8)\n"
    "v_mfma_f32_16x16x32_bf16 v[180:183], a[36:39], v[28:31], v[180:183]\n"
    "ds_read_b64_tr_b16 a[14:15], v199 offset:2560\n"
    "v_cndmask_b32 v236, v236, v237, vcc\n"
    "s_barrier\n"
    "v_mfma_f32_16x16x32_bf16 v[180:183], a[40:43], v[40:43], v[180:183]\n"
    "ds_read_b64_tr_b16 a[16:17], v192 offset:4096\n"
    "v_cndmask_b32 v238, v238, v239, vcc\n"
    "v_mfma_f32_16x16x32_bf16 v[180:183], a[44:47], v[44:47], v[180:183]\n"
    "ds_read_b64_tr_b16 a[18:19], v192 offset:4608\n"
    "v_cndmask_b32 v236, v236, v238, s[0:1]\n"
    "v_mfma_f32_16x16x32_bf16 v[176:179], a[40:43], v[32:35], v[176:179]\n"
    "ds_read_b64_tr_b16 a[20:21], v196 offset:4096\n"
    "ds_bpermute_b32 v237, v252, v236\n"
    "v_mfma_f32_16x16x32_bf16 v[176:179], a[44:47], v[36:39], v[176:179]\n"
    "ds_read_b64_tr_b16 a[22:23], v196 offset:4608\n"
    "s_waitcnt lgkmcnt(5)\n"
    "v_mfma_f32_16x16x32_bf16 v[184:187], a[0:3], v[16:19], 0\n"
    "ds_read_b64_tr_b16 a[24:25], v192 offset:6144\n"
    "s_add_u32 m0, s78, 20480\n"
    "v_mfma_f32_16x16x32_bf16 v[184:187], a[4:7], v[20:23], v[184:187]\n"
    "ds_read_b64_tr_b16 a[26:27], v192 offset:6656\n"
    "s_mul_i32 s83, s93, s77\n"
    "v_mfma_f32_16x16x32_bf16 v[188:191], a[0:3], v[24:27], 0\n"
    "ds_read_b64_tr_b16 a[28:29], v196 offset:6144\n"
    "buffer_load_dwordx4 v10, s[36:39], s83 offen lds\n"
    "v_mfma_f32_16x16x32_bf16 v[188:191], a[4:7], v[28:31], v[188:191]\n"
    "ds_read_b64_tr_b16 a[30:31], v196 offset:6656\n"
    "s_lshl_b32 s84, s69, 7\n"
    "v_mfma_f32_16x16x32_bf16 v[188:191], a[8:11], v[40:43], v[188:191]\n"
    "ds_read_b64_tr_b16 a[32:33], v193 offset:4096\n"
    "v_mfma_f32_16x16x32_bf16 v[188:191], a[12:15], v[44:47], v[188:191]\n"
    "ds_read_b64_tr_b16 a[34:35], v193 offset:4608\n"
    "buffer_load_dwordx4 v11, s[36:39], s83 offen offset:1024 lds\n"
    "v_mfma_f32_16x16x32_bf16 v[184:187], a[8:11], v[32:35], v[184:187]\n"
    "ds_read_b64_tr_b16 a[36:37], v197 offset:4096\n"
    "s_cmp_eq_u32 s89, 1\n"
    "s_cselect_b32 s81, 0x3e8b97f6, 1.0\n"
    "v_mfma_f32_16x16x32_bf16 v[184:187], a[12:15], v[36:39], v[184:187]\n"
    "ds_read_b64_tr_b16 a[38:39], v197 offset:4608\n"
    "s_waitcnt lgkmcnt(4)\n"
    "v_mfma_f32_16x16x32_bf16 v[160:163], a[16:19], v[48:51], v[160:163]\n"
    "ds_read_b64_tr_b16 a[40:41], v193 offset:6144\n"
    "buffer_atomic_add v201, v216, s[8:11], s84 offen sc0\n"
    "v_mfma_f32_16x16x32_bf16 v[160:163], a[20:23], v[52:55], v[160:163]\n"
    "ds_read_b64_tr_b16 a[42:43], v193 offset:6656\n"
    "v_mov_b32 v245, s81\n"
    "v_mfma_f32_16x16x32_bf16 v[164:167], a[16:19], v[56:59], v[164:167]\n"
    "ds_read_b64_tr_b16 a[44:45], v197 offset:6144\n"
    "s_cmp_eq_u32 s89, 0\n"
    "s_cselect_b64 s[0:1], -1, 0\n"
    "v_mfma_f32_16x16x32_bf16 v[164:167], a[20:23], v[60:63], v[164:167]\n"
    "ds_read_b64_tr_b16 a[46:47], v197 offset:6656\n"
    "v_mul_f32 v233, 0x3b124925, v233\n"
    "v_mfma_f32_16x16x32_bf16 v[164:167], a[24:27], v[72:75], v[164:167]\n"
    "ds_read_b64_tr_b16 a[0:1], v194 offset:4096\n"
    "s_add_u32 m0, s78, 22528\n"
    "v_mfma_f32_16x16x32_bf16 v[164:167], a[28:31], v[76:79], v[164:167]\n"
    "ds_read_b64_tr_b16 a[2:3], v194 offset:4608\n"
    "s_mul_i32 s83, s94, s77\n"
    "v_mfma_f32_16x16x32_bf16 v[160:163], a[24:27], v[64:67], v[160:163]\n"
    "ds_read_b64_tr_b16 a[4:5], v198 offset:4096\n"
    "buffer_load_dwordx4 v10, s[36:39], s83 offen lds\n"
    "v_mfma_f32_16x16x32_bf16 v[160:163], a[28:31], v[68:71], v[160:163]\n"
    "ds_read_b64_tr_b16 a[6:7], v198 offset:4608\n"
    "s_waitcnt lgkmcnt(4)\n"
    "v_mfma_f32_16x16x32_bf16 v[168:171], a[32:35], v[48:51], v[168:171]\n"
    "ds_read_b64_tr_b16 a[8:9], v194 offset:6144\n"
    "v_fma_f32 v233, -v224, v224, v233\n"
    "v_mfma_f32_16x16x32_bf16 v[168:171], a[36:39], v[52:55], v[168:171]\n"
    "ds_read_b64_tr_b16 a[10:11], v194 offset:6656\n"
    "buffer_load_dwordx4 v11, s[36:39], s83 offen offset:1024 lds\n"
    "v_mfma_f32_16x16x32_bf16 v[172:175], a[32:35], v[56:59], v[172:175]\n"
    "ds_read_b64_tr_b16 a[12:13], v198 offset:6144\n"
    "v_max_f32 v233, 0, v233\n"
    "v_mfma_f32_16x16x32_bf16 v[172:175], a[36:39], v[60:63], v[172:175]\n"
    "ds_read_b64_tr_b16 a[14:15], v198 offset:6656\n"
    "v_add_f32 v233, 0x3727c5ac, v233\n"
    "v_mfma_f32_16x16x32_bf16 v[172:175], a[40:43], v[72:75], v[172:175]\n"
    "ds_read_b64_tr_b16 a[16:17], v195 offset:4096\n"
    "v_rsq_f32 v200, v233\n"
    "v_mfma_f32_16x16x32_bf16 v[172:175], a[44:47], v[76:79], v[172:175]\n"
    "ds_read_b64_tr_b16 a[18:19], v195 offset:4608\n"
    "v_cndmask_b32 v246, v245, v200, s[0:1]\n"
    "v_mfma_f32_16x16x32_bf16 v[168:171], a[40:43], v[64:67], v[168:171]\n"
    "ds_read_b64_tr_b16 a[20:21], v199 offset:4096\n"
    "v_mul_f32 v249, v246, v200\n"
    "v_mfma_f32_16x16x32_bf16 v[168:171], a[44:47], v[68:71], v[168:171]\n"
    "ds_read_b64_tr_b16 a[22:23], v199 offset:4608\n"
    "s_waitcnt lgkmcnt(4)\n"
    "v_mfma_f32_16x16x32_bf16 v[176:179], a[0:3], v[48:51], v[176:179]\n"
    "ds_read_b64_tr_b16 a[24:25], v195 offset:6144\n"
    "s_add_u32 m0, s78, 24576\n"
    "v_mfma_f32_16x16x32_bf16 v[176:179], a[4:7], v[52:55], v[176:179]\n"
    "ds_read_b64_tr_b16 a[26:27], v195 offset:6656\n"
    "s_mul_i32 s83, s95, s77\n"
    "v_mfma_f32_16x16x32_bf16 v[180:183], a[0:3], v[56:59], v[180:183]\n"
    "ds_read_b64_tr_b16 a[28:29], v199 offset:6144\n"
    "buffer_load_dwordx4 v10, s[36:39], s83 offen lds\n"
    "v_mfma_f32_16x16x32_bf16 v[180:183], a[4:7], v[60:63], v[180:183]\n"
    "ds_read_b64_tr_b16 a[30:31], v199 offset:6656\n"
    "v_mul_f32 v248, 0x3e8b97f6, v200\n"
    "v_mfma_f32_16x16x32_bf16 v[180:183], a[8:11], v[72:75], v[180:183]\n"
    "ds_read_b64_tr_b16 a[32:33], v192 offset:8192\n"
    "v_mul_f32 v200, v200, v200\n"
    "v_mfma_f32_16x16x32_bf16 v[180:183], a[12:15], v[76:79], v[180:183]\n"
    "ds_read_b64_tr_b16 a[34:35], v192 offset:8704\n"
    "buffer_load_dwordx4 v11, s[36:39], s83 offen offset:1024 lds\n"
    "v_mfma_f32_16x16x32_bf16 v[176:179], a[8:11], v[64:67], v[176:179]\n"
    "ds_read_b64_tr_b16 a[36:37], v196 offset:8192\n"
    "v_mul_f32 v237, 0x3b124925, v237\n"
    "v_mfma_f32_16x16x32_bf16 v[176:179], a[12:15], v[68:71], v[176:179]\n"
    "ds_read_b64_tr_b16 a[38:39], v196 offset:8704\n"
    "s_waitcnt lgkmcnt(4)\n"
    "v_mfma_f32_16x16x32_bf16 v[184:187], a[16:19], v[48:51], v[184:187]\n"
    "ds_read_b64_tr_b16 a[40:41], v192 offset:10240\n"
    "v_fma_f32 v237, -v228, v228, v237\n"
    "v_mfma_f32_16x16x32_bf16 v[184:187], a[20:23], v[52:55], v[184:187]\n"
    "ds_read_b64_tr_b16 a[42:43], v192 offset:10752\n"
    "v_max_f32 v237, 0, v237\n"
    "v_mfma_f32_16x16x32_bf16 v[188:191], a[16:19], v[56:59], v[188:191]\n"
    "ds_read_b64_tr_b16 a[44:45], v196 offset:10240\n"
    "v_add_f32 v237, 0x3727c5ac, v237\n"
    "v_mfma_f32_16x16x32_bf16 v[188:191], a[20:23], v[60:63], v[188:191]\n"
    "ds_read_b64_tr_b16 a[46:47], v196 offset:10752\n"
    "v_rsq_f32 v202, v237\n"
    "v_mfma_f32_16x16x32_bf16 v[188:191], a[24:27], v[72:75], v[188:191]\n"
    "ds_read_b64_tr_b16 a[0:1], v193 offset:8192\n"
    "s_add_u32 m0, s78, 26624\n"
    "v_mfma_f32_16x16x32_bf16 v[188:191], a[28:31], v[76:79], v[188:191]\n"
    "ds_read_b64_tr_b16 a[2:3], v193 offset:8704\n"
    "s_mul_i32 s83, s96, s77\n"
    "v_mfma_f32_16x16x32_bf16 v[184:187], a[24:27], v[64:67], v[184:187]\n"
    "ds_read_b64_tr_b16 a[4:5], v197 offset:8192\n"
    "buffer_load_dwordx4 v10, s[36:39], s83 offen lds\n"
    "v_mfma_f32_16x16x32_bf16 v[184:187], a[28:31], v[68:71], v[184:187]\n"
    "ds_read_b64_tr_b16 a[6:7], v197 offset:8704\n"
    "s_waitcnt lgkmcnt(4)\n"
    "v_mfma_f32_16x16x32_bf16 v[160:163], a[32:35], v[80:83], v[160:163]\n"
    "ds_read_b64_tr_b16 a[8:9], v193 offset:10240\n"
    "v_cndmask_b32 v246, v245, v202, s[0:1]\n"
    "v_mfma_f32_16x16x32_bf16 v[160:163], a[36:39], v[84:87], v[160:163]\n"
    "ds_read_b64_tr_b16 a[10:11], v193 offset:10752\n"
    "buffer_load_dwordx4 v11, s[36:39], s83 offen offset:1024 lds\n"
    "v_mfma_f32_16x16x32_bf16 v[164:167], a[32:35], v[88:91], v[164:167]\n"
    "ds_read_b64_tr_b16 a[12:13], v197 offset:10240\n"
    "v_mul_f32 v251, v246, v202\n"
    "v_mfma_f32_16x16x32_bf16 v[164:167], a[36:39], v[92:95], v[164:167]\n"
    "ds_read_b64_tr_b16 a[14:15], v197 offset:10752\n"
    "v_mul_f32 v250, 0x3e8b97f6, v202\n"
    "v_mfma_f32_16x16x32_bf16 v[164:167], a[40:43], v[104:107], v[164:167]\n"
    "ds_read_b64_tr_b16 a[16:17], v194 offset:8192\n"
    "v_mul_f32 v202, v202, v202\n"
    "v_mfma_f32_16x16x32_bf16 v[164:167], a[44:47], v[108:111], v[164:167]\n"
    "ds_read_b64_tr_b16 a[18:19], v194 offset:8704\n"
    "s_mul_i32 s81, s71, 112\n"
    "v_mfma_f32_16x16x32_bf16 v[160:163], a[40:43], v[96:99], v[160:163]\n"
    "ds_read_b64_tr_b16 a[20:21], v198 offset:8192\n"
    "s_lshl_b32 s62, s74, 5\n"
    "v_mfma_f32_16x16x32_bf16 v[160:163], a[44:47], v[100:103], v[160:163]\n"
    "ds_read_b64_tr_b16 a[22:23], v198 offset:8704\n"
    "s_waitcnt lgkmcnt(4)\n"
    "v_mfma_f32_16x16x32_bf16 v[168:171], a[0:3], v[80:83], v[168:171]\n"
    "ds_read_b64_tr_b16 a[24:25], v194 offset:10240\n"
    "s_add_u32 m0, s78, 28672\n"
    "v_mfma_f32_16x16x32_bf16 v[168:171], a[4:7], v[84:87], v[168:171]\n"
    "ds_read_b64_tr_b16 a[26:27], v194 offset:10752\n"
    "s_mul_i32 s83, s90, 3584\n"
    "v_mfma_f32_16x16x32_bf16 v[172:175], a[0:3], v[88:91], v[172:175]\n"
    "ds_read_b64_tr_b16 a[28:29], v198 offset:10240\n"
    "buffer_load_dwordx4 v14, s[16:19], s83 offen lds\n"
    "v_mfma_f32_16x16x32_bf16 v[172:175], a[4:7], v[92:95], v[172:175]\n"
    "ds_read_b64_tr_b16 a[30:31], v198 offset:10752\n"
    "s_add_u32 s81, s81, s62\n"
    "v_mfma_f32_16x16x32_bf16 v[172:175], a[8:11], v[104:107], v[172:175]\n"
    "ds_read_b64_tr_b16 a[32:33], v195 offset:8192\n"
    "s_lshl2_add_u32 s81, s81, s68\n"
    "v_mfma_f32_16x16x32_bf16 v[172:175], a[12:15], v[108:111], v[172:175]\n"
    "ds_read_b64_tr_b16 a[34:35], v195 offset:8704\n"
    "buffer_load_dwordx4 v15, s[16:19], s83 offen offset:1024 lds\n"
    "v_mfma_f32_16x16x32_bf16 v[168:171], a[8:11], v[96:99], v[168:171]\n"
    "ds_read_b64_tr_b16 a[36:37], v199 offset:8192\n"
    "s_lshl_b32 s81, s81, 8\n"
    "v_mfma_f32_16x16x32_bf16 v[168:171], a[12:15], v[100:103], v[168:171]\n"
    "ds_read_b64_tr_b16 a[38:39], v199 offset:8704\n"
    "s_waitcnt lgkmcnt(4)\n"
    "v_mfma_f32_16x16x32_bf16 v[176:179], a[16:19], v[80:83], v[176:179]\n"
    "ds_read_b64_tr_b16 a[40:41], v195 offset:10240\n"
    "s_add_u32 s62, s81, 524288\n"
    "v_mfma_f32_16x16x32_bf16 v[176:179], a[20:23], v[84:87], v[176:179]\n"
    "ds_read_b64_tr_b16 a[42:43], v195 offset:10752\n"
    "s_lshl_b32 s53, s63, 12\n"
    "v_mfma_f32_16x16x32_bf16 v[180:183], a[16:19], v[88:91], v[180:183]\n"
    "ds_read_b64_tr_b16 a[44:45], v199 offset:10240\n"
    "s_waitcnt vmcnt(6)\n"
    "v_mfma_f32_16x16x32_bf16 v[180:183], a[20:23], v[92:95], v[180:183]\n"
    "ds_read_b64_tr_b16 a[46:47], v199 offset:10752\n"
    "s_add_u32 s53, s53, 131072\n"
    "s_barrier\n"
    "v_mfma_f32_16x16x32_bf16 v[180:183], a[24:27], v[104:107], v[180:183]\n"
    "ds_read_b64_tr_b16 a[0:1], v192 offset:12288\n"
    "s_add_u32 m0, s78, 30720\n"
    "v_mfma_f32_16x16x32_bf16 v[180:183], a[28:31], v[108:111], v[180:183]\n"
    "ds_read_b64_tr_b16 a[2:3], v192 offset:12800\n"
    "s_mul_i32 s83, s91, 3584\n"
    "v_mfma_f32_16x16x32_bf16 v[176:179], a[24:27], v[96:99], v[176:179]\n"
    "ds_read_b64_tr_b16 a[4:5], v196 offset:12288\n"
    "buffer_load_dwordx4 v14, s[16:19], s83 offen lds\n"
    "v_mfma_f32_16x16x32_bf16 v[176:179], a[28:31], v[100:103], v[176:179]\n"
    "ds_read_b64_tr_b16 a[6:7], v196 offset:12800\n"
    "s_waitcnt lgkmcnt(4)\n"
    "v_mfma_f32_16x16x32_bf16 v[184:187], a[32:35], v[80:83], v[184:187]\n"
    "ds_read_b64_tr_b16 a[8:9], v193 offset:12288\n"
    "s_lshl2_add_u32 s55, s2, s68\n"
    "v_mfma_f32_16x16x32_bf16 v[184:187], a[36:39], v[84:87], v[184:187]\n"
    "ds_read_b64_tr_b16 a[10:11], v193 offset:12800\n"
    "buffer_load_dwordx4 v15, s[16:19], s83 offen offset:1024 lds\n"
    "v_mfma_f32_16x16x32_bf16 v[188:191], a[32:35], v[88:91], v[188:191]\n"
    "ds_read_b64_tr_b16 a[12:13], v197 offset:12288\n"
    "s_mul_i32 s3, s55, 1664\n"
    "v_mfma_f32_16x16x32_bf16 v[188:191], a[36:39], v[92:95], v[188:191]\n"
    "ds_read_b64_tr_b16 a[14:15], v197 offset:12800\n"
    "s_lshl_b32 s55, s55, 9\n"
    "v_mfma_f32_16x16x32_bf16 v[188:191], a[40:43], v[104:107], v[188:191]\n"
    "ds_read_b64_tr_b16 a[16:17], v194 offset:12288\n"
    "v_lshlrev_b32 v212, 7, v0\n"
    "v_mfma_f32_16x16x32_bf16 v[188:191], a[44:47], v[108:111], v[188:191]\n"
    "ds_read_b64_tr_b16 a[18:19], v194 offset:12800\n"
    "s_mov_b64 exec, 8191\n"
    "buffer_load_dword a100, v212, s[20:23], s3 offen\n"
    "s_mov_b64 exec, -1\n"
    "v_mfma_f32_16x16x32_bf16 v[184:187], a[40:43], v[96:99], v[184:187]\n"
    "ds_read_b64_tr_b16 a[20:21], v198 offset:12288\n"
    "s_mov_b64 exec, 15\n"
    "buffer_load_dword a101, v212, s[24:27], s55 offen\n"
    "s_mov_b64 exec, -1\n"
    "v_mfma_f32_16x16x32_bf16 v[184:187], a[44:47], v[100:103], v[184:187]\n"
    "ds_read_b64_tr_b16 a[22:23], v198 offset:12800\n"
    "s_waitcnt lgkmcnt(4)\n"
    "v_mfma_f32_16x16x32_bf16 v[160:163], a[0:3], v[112:115], v[160:163]\n"
    "ds_read_b64_tr_b16 a[24:25], v195 offset:12288\n"
    "s_add_u32 m0, s78, 32768\n"
    "v_mfma_f32_16x16x32_bf16 v[160:163], a[4:7], v[116:119], v[160:163]\n"
    "ds_read_b64_tr_b16 a[26:27], v195 offset:12800\n"
    "s_mul_i32 s83, s92, 3584\n"
    "v_mfma_f32_16x16x32_bf16 v[164:167], a[0:3], v[120:123], v[164:167]\n"
    "ds_read_b64_tr_b16 a[28:29], v199 offset:12288\n"
    "buffer_load_dwordx4 v14, s[16:19], s83 offen lds\n"
    "v_mfma_f32_16x16x32_bf16 v[164:167], a[4:7], v[124:127], v[164:167]\n"
    "ds_read_b64_tr_b16 a[30:31], v199 offset:12800\n"
    "s_add_u32 s52, s62, 16384\n"
    "v_mfma_f32_16x16x32_bf16 v[168:171], a[8:11], v[112:115], v[168:171]\n"
    "ds_read_b64_tr_b16 a[32:33], v2 offset:14336\n"
    "s_cmp_eq_u32 s89, 2\n"
    "s_cselect_b32 s52, s53, s52\n"
    "s_cselect_b32 s53, 0, 3840\n"
    "s_cselect_b32 s54, 0, 3072\n"
    "v_mfma_f32_16x16x32_bf16 v[168:171], a[12:15], v[116:119], v[168:171]\n"
    "ds_read_b64_tr_b16 a[34:35], v2 offset:14848\n"
    "buffer_load_dwordx4 v15, s[16:19], s83 offen offset:1024 lds\n"
    "v_mfma_f32_16x16x32_bf16 v[172:175], a[8:11], v[120:123], v[172:175]\n"
    "ds_read_b64_tr_b16 a[36:37], v6 offset:14336\n"
    "s_add_u32 s53, s53, s52\n"
    "v_mfma_f32_16x16x32_bf16 v[172:175], a[12:15], v[124:127], v[172:175]\n"
    "ds_read_b64_tr_b16 a[38:39], v6 offset:14848\n"
    "s_waitcnt lgkmcnt(4)\n"
    "v_mfma_f32_16x16x32_bf16 v[176:179], a[16:19], v[112:115], v[176:179]\n"
    "ds_read_b64_tr_b16 a[40:41], v3 offset:14336\n"
    "buffer_load_dword v220, v218, s[8:11], 0 offen sc1\n"
    "v_mfma_f32_16x16x32_bf16 v[176:179], a[20:23], v[116:119], v[176:179]\n"
    "ds_read_b64_tr_b16 a[42:43], v3 offset:14848\n"
    "s_add_u32 s54, s54, s52\n"
    "v_mfma_f32_16x16x32_bf16 v[180:183], a[16:19], v[120:123], v[180:183]\n"
    "ds_read_b64_tr_b16 a[44:45], v7 offset:14336\n"
    "s_lshl4_add_u32 s59, s63, 81920\n"
    "v_mfma_f32_16x16x32_bf16 v[180:183], a[20:23], v[124:127], v[180:183]\n"
    "ds_read_b64_tr_b16 a[46:47], v7 offset:14848\n"
    "v_mul_f32 v160, v249, v160\n"
    "v_mfma_f32_16x16x32_bf16 v[184:187], a[24:27], v[112:115], v[184:187]\n"
    "ds_read_b64_tr_b16 a[0:1], v4 offset:14336\n"
    "s_add_u32 m0, s78, 34816\n"
    "v_mfma_f32_16x16x32_bf16 v[184:187], a[28:31], v[116:119], v[184:187]\n"
    "ds_read_b64_tr_b16 a[2:3], v4 offset:14848\n"
    "s_mul_i32 s83, s93, 3584\n"
    "v_mfma_f32_16x16x32_bf16 v[188:191], a[24:27], v[120:123], v[188:191]\n"
    "ds_read_b64_tr_b16 a[4:5], v8 offset:14336\n"
    "buffer_load_dwordx4 v14, s[16:19], s83 offen lds\n"
    "v_mfma_f32_16x16x32_bf16 v[188:191], a[28:31], v[124:127], v[188:191]\n"
    "ds_read_b64_tr_b16 a[6:7], v8 offset:14848\n"
    "s_waitcnt lgkmcnt(4)\n"
    "v_mfma_f32_16x16x32_bf16 v[128:131], a[32:35], v[16:19], 0\n"
    "ds_read_b64_tr_b16 a[8:9], v5 offset:14336\n"
    "v_mul_f32 v161, v249, v161\n"
    "v_mfma_f32_16x16x32_bf16 v[128:131], a[36:39], v[20:23], v[128:131]\n"
    "ds_read_b64_tr_b16 a[10:11], v5 offset:14848\n"
    "buffer_load_dwordx4 v15, s[16:19], s83 offen offset:1024 lds\n"
    "v_mfma_f32_16x16x32_bf16 v[132:135], a[32:35], v[24:27], 0\n"
    "ds_read_b64_tr_b16 a[12:13], v9 offset:14336\n"
    "v_mul_f32 v162, v249, v162\n"
    "v_mfma_f32_16x16x32_bf16 v[132:135], a[36:39], v[28:31], v[132:135]\n"
    "ds_read_b64_tr_b16 a[14:15], v9 offset:14848\n"
    "v_mul_f32 v163, v249, v163\n"
    "v_mfma_f32_16x16x32_bf16 v[136:139], a[40:43], v[16:19], 0\n"
    "ds_read_b64_tr_b16 a[16:17], v2 offset:16384\n"
    "v_mul_f32 v164, v251, v164\n"
    "v_mfma_f32_16x16x32_bf16 v[136:139], a[44:47], v[20:23], v[136:139]\n"
    "ds_read_b64_tr_b16 a[18:19], v2 offset:16896\n"
    "v_mul_f32 v165, v251, v165\n"
    "v_mfma_f32_16x16x32_bf16 v[140:143], a[40:43], v[24:27], 0\n"
    "ds_read_b64_tr_b16 a[20:21], v6 offset:16384\n"
    "v_mul_f32 v166, v251, v166\n"
    "v_mfma_f32_16x16x32_bf16 v[140:143], a[44:47], v[28:31], v[140:143]\n"
    "ds_read_b64_tr_b16 a[22:23], v6 offset:16896\n"
    "s_waitcnt lgkmcnt(4)\n"
    "v_mfma_f32_16x16x32_bf16 v[144:147], a[0:3], v[16:19], 0\n"
    "ds_read_b64_tr_b16 a[24:25], v2 offset:18432\n"
    "s_add_u32 m0, s78, 0\n"
    "v_mfma_f32_16x16x32_bf16 v[144:147], a[4:7], v[20:23], v[144:147]\n"
    "ds_read_b64_tr_b16 a[26:27], v2 offset:18944\n"
    "s_mul_i32 s83, s94, 3584\n"
    "v_mfma_f32_16x16x32_bf16 v[148:151], a[0:3], v[24:27], 0\n"
    "ds_read_b64_tr_b16 a[28:29], v6 offset:18432\n"
    "buffer_load_dwordx4 v14, s[16:19], s83 offen lds\n"
    "v_mfma_f32_16x16x32_bf16 v[148:151], a[4:7], v[28:31], v[148:151]\n"
    "ds_read_b64_tr_b16 a[30:31], v6 offset:18944\n"
    "v_mul_f32 v167, v251, v167\n"
    "v_mfma_f32_16x16x32_bf16 v[152:155], a[8:11], v[16:19], 0\n"
    "ds_read_b64_tr_b16 a[32:33], v3 offset:16384\n"
    "v_cvt_pk_bf16_f32 v224, v160, v161\n"
    "v_cvt_pk_bf16_f32 v225, v162, v163\n"
    "v_mfma_f32_16x16x32_bf16 v[152:155], a[12:15], v[20:23], v[152:155]\n"
    "ds_read_b64_tr_b16 a[34:35], v3 offset:16896\n"
    "buffer_load_dwordx4 v15, s[16:19], s83 offen offset:1024 lds\n"
    "v_mfma_f32_16x16x32_bf16 v[156:159], a[8:11], v[24:27], 0\n"
    "ds_read_b64_tr_b16 a[36:37], v7 offset:16384\n"
    "v_cvt_pk_bf16_f32 v226, v164, v165\n"
    "v_cvt_pk_bf16_f32 v227, v166, v167\n"
    "v_mfma_f32_16x16x32_bf16 v[156:159], a[12:15], v[28:31], v[156:159]\n"
    "ds_read_b64_tr_b16 a[38:39], v7 offset:16896\n"
    "s_waitcnt lgkmcnt(4)\n"
    "v_mfma_f32_16x16x32_bf16 v[128:131], a[16:19], v[32:35], v[128:131]\n"
    "ds_read_b64_tr_b16 a[40:41], v3 offset:18432\n"
    "v_mul_f32 v168, v249, v168\n"
    "v_mul_f32 v169, v249, v169\n"
    "v_mfma_f32_16x16x32_bf16 v[128:131], a[20:23], v[36:39], v[128:131]\n"
    "ds_read_b64_tr_b16 a[42:43], v3 offset:18944\n"
    "v_mul_f32 v170, v249, v170\n"
    "v_mul_f32 v171, v249, v171\n"
    "v_mfma_f32_16x16x32_bf16 v[132:135], a[16:19], v[40:43], v[132:135]\n"
    "ds_read_b64_tr_b16 a[44:45], v7 offset:18432\n"
    "v_mul_f32 v172, v251, v172\n"
    "v_mul_f32 v173, v251, v173\n"
    "v_mfma_f32_16x16x32_bf16 v[132:135], a[20:23], v[44:47], v[132:135]\n"
    "ds_read_b64_tr_b16 a[46:47], v7 offset:18944\n"
    "v_mul_f32 v174, v251, v174\n"
    "v_mul_f32 v175, v251, v175\n"
    "v_mfma_f32_16x16x32_bf16 v[132:135], a[24:27], v[56:59], v[132:135]\n"
    "ds_read_b64_tr_b16 a[0:1], v4 offset:16384\n"
    "s_add_u32 m0, s78, 2048\n"
    "v_mfma_f32_16x16x32_bf16 v[132:135], a[28:31], v[60:63], v[132:135]\n"
    "ds_read_b64_tr_b16 a[2:3], v4 offset:16896\n"
    "s_mul_i32 s83, s95, 3584\n"
    "v_mfma_f32_16x16x32_bf16 v[128:131], a[24:27], v[48:51], v[128:131]\n"
    "ds_read_b64_tr_b16 a[4:5], v8 offset:16384\n"
    "buffer_load_dwordx4 v14, s[16:19], s83 offen lds\n"
    "v_mfma_f32_16x16x32_bf16 v[128:131], a[28:31], v[52:55], v[128:131]\n"
    "ds_read_b64_tr_b16 a[6:7], v8 offset:16896\n"
    "s_waitcnt lgkmcnt(4)\n"
    "v_mfma_f32_16x16x32_bf16 v[136:139], a[32:35], v[32:35], v[136:139]\n"
    "ds_read_b64_tr_b16 a[8:9], v4 offset:18432\n"
    "v_cvt_pk_bf16_f32 v228, v168, v169\n"
    "v_cvt_pk_bf16_f32 v229, v170, v171\n"
    "v_mfma_f32_16x16x32_bf16 v[136:139], a[36:39], v[36:39], v[136:139]\n"
    "ds_read_b64_tr_b16 a[10:11], v4 offset:18944\n"
    "buffer_load_dwordx4 v15, s[16:19], s83 offen offset:1024 lds\n"
    "v_mfma_f32_16x16x32_bf16 v[140:143], a[32:35], v[40:43], v[140:143]\n"
    "ds_read_b64_tr_b16 a[12:13], v8 offset:18432\n"
    "v_cvt_pk_bf16_f32 v230, v172, v173\n"
    "v_cvt_pk_bf16_f32 v231, v174, v175\n"
    "v_mfma_f32_16x16x32_bf16 v[140:143], a[36:39], v[44:47], v[140:143]\n"
    "ds_read_b64_tr_b16 a[14:15], v8 offset:18944\n"
    "v_mul_f32 v176, v249, v176\n"
    "v_mul_f32 v177, v249, v177\n"
    "v_mfma_f32_16x16x32_bf16 v[140:143], a[40:43], v[56:59], v[140:143]\n"
    "ds_read_b64_tr_b16 a[16:17], v5 offset:16384\n"
    "v_mul_f32 v178, v249, v178\n"
    "v_mul_f32 v179, v249, v179\n"
    "v_mfma_f32_16x16x32_bf16 v[140:143], a[44:47], v[60:63], v[140:143]\n"
    "ds_read_b64_tr_b16 a[18:19], v5 offset:16896\n"
    "v_mul_f32 v180, v251, v180\n"
    "v_mul_f32 v181, v251, v181\n"
    "v_mfma_f32_16x16x32_bf16 v[136:139], a[40:43], v[48:51], v[136:139]\n"
    "ds_read_b64_tr_b16 a[20:21], v9 offset:16384\n"
    "v_mul_f32 v182, v251, v182\n"
    "v_mul_f32 v183, v251, v183\n"
    "v_mfma_f32_16x16x32_bf16 v[136:139], a[44:47], v[52:55], v[136:139]\n"
    "ds_read_b64_tr_b16 a[22:23], v9 offset:16896\n"
    "s_waitcnt lgkmcnt(4)\n"
    "v_mfma_f32_16x16x32_bf16 v[144:147], a[0:3], v[32:35], v[144:147]\n"
    "ds_read_b64_tr_b16 a[24:25], v5 offset:18432\n"
    "s_add_u32 m0, s78, 4096\n"
    "v_mfma_f32_16x16x32_bf16 v[144:147], a[4:7], v[36:39], v[144:147]\n"
    "ds_read_b64_tr_b16 a[26:27], v5 offset:18944\n"
    "s_mul_i32 s83, s96, 3584\n"
    "v_mfma_f32_16x16x32_bf16 v[148:151], a[0:3], v[40:43], v[148:151]\n"
    "ds_read_b64_tr_b16 a[28:29], v9 offset:18432\n"
    "buffer_load_dwordx4 v14, s[16:19], s83 offen lds\n"
    "v_mfma_f32_16x16x32_bf16 v[148:151], a[4:7], v[44:47], v[148:151]\n"
    "ds_read_b64_tr_b16 a[30:31], v9 offset:18944\n"
    "v_cvt_pk_bf16_f32 v232, v176, v177\n"
    "v_cvt_pk_bf16_f32 v233, v178, v179\n"
    "v_mfma_f32_16x16x32_bf16 v[148:151], a[8:11], v[56:59], v[148:151]\n"
    "ds_read_b64_tr_b16 a[32:33], v2 offset:20480\n"
    "v_cvt_pk_bf16_f32 v234, v180, v181\n"
    "v_cvt_pk_bf16_f32 v235, v182, v183\n"
    "v_mfma_f32_16x16x32_bf16 v[148:151], a[12:15], v[60:63], v[148:151]\n"
    "ds_read_b64_tr_b16 a[34:35], v2 offset:20992\n"
    "buffer_load_dwordx4 v15, s[16:19], s83 offen offset:1024 lds\n"
    "v_mfma_f32_16x16x32_bf16 v[144:147], a[8:11], v[48:51], v[144:147]\n"
    "ds_read_b64_tr_b16 a[36:37], v6 offset:20480\n"
    "v_mul_f32 v184, v249, v184\n"
    "v_mul_f32 v185, v249, v185\n"
    "v_mfma_f32_16x16x32_bf16 v[144:147], a[12:15], v[52:55], v[144:147]\n"
    "ds_read_b64_tr_b16 a[38:39], v6 offset:20992\n"
    "s_waitcnt lgkmcnt(4)\n"
    "v_mfma_f32_16x16x32_bf16 v[152:155], a[16:19], v[32:35], v[152:155]\n"
    "ds_read_b64_tr_b16 a[40:41], v2 offset:22528\n"
    "v_mul_f32 v186, v249, v186\n"
    "v_mul_f32 v187, v249, v187\n"
    "v_mfma_f32_16x16x32_bf16 v[152:155], a[20:23], v[36:39], v[152:155]\n"
    "ds_read_b64_tr_b16 a[42:43], v2 offset:23040\n"
    "v_mul_f32 v188, v251, v188\n"
    "v_mul_f32 v189, v251, v189\n"
    "v_mfma_f32_16x16x32_bf16 v[156:159], a[16:19], v[40:43], v[156:159]\n"
    "ds_read_b64_tr_b16 a[44:45], v6 offset:22528\n"
    "v_mul_f32 v190, v251, v190\n"
    "v_mul_f32 v191, v251, v191\n"
    "v_mfma_f32_16x16x32_bf16 v[156:159], a[20:23], v[44:47], v[156:159]\n"
    "ds_read_b64_tr_b16 a[46:47], v6 offset:23040\n"
    "v_cvt_pk_bf16_f32 v236, v184, v185\n"
    "v_cvt_pk_bf16_f32 v237, v186, v187\n"
    "v_mfma_f32_16x16x32_bf16 v[156:159], a[24:27], v[56:59], v[156:159]\n"
    "ds_read_b64_tr_b16 a[0:1], v3 offset:20480\n"
    "s_add_u32 m0, s78, 6144\n"
    "v_mfma_f32_16x16x32_bf16 v[156:159], a[28:31], v[60:63], v[156:159]\n"
    "ds_read_b64_tr_b16 a[2:3], v3 offset:20992\n"
    "s_mul_i32 s83, s90, 3584\n"
    "v_mfma_f32_16x16x32_bf16 v[152:155], a[24:27], v[48:51], v[152:155]\n"
    "ds_read_b64_tr_b16 a[4:5], v7 offset:20480\n"
    "buffer_load_dwordx4 v14, s[12:15], s83 offen lds\n"
    "v_mfma_f32_16x16x32_bf16 v[152:155], a[28:31], v[52:55], v[152:155]\n"
    "ds_read_b64_tr_b16 a[6:7], v7 offset:20992\n"
    "s_waitcnt lgkmcnt(4)\n"
    "v_mfma_f32_16x16x32_bf16 v[128:131], a[32:35], v[64:67], v[128:131]\n"
    "ds_read_b64_tr_b16 a[8:9], v3 offset:22528\n"
    "s_waitcnt vmcnt(9)\n"
    "v_mfma_f32_16x16x32_bf16 v[128:131], a[36:39], v[68:71], v[128:131]\n"
    "ds_read_b64_tr_b16 a[10:11], v3 offset:23040\n"
    "buffer_load_dwordx4 v15, s[12:15], s83 offen offset:1024 lds\n"
    ".Lpage_2_%=:\n"
    "s_cbranch_execz .Lpage_6_%=\n"
    "v_mfma_f32_16x16x32_bf16 v[132:135], a[32:35], v[72:75], v[132:135]\n"
    "ds_read_b64_tr_b16 a[12:13], v7 offset:22528\n"
    "v_mfma_f32_16x16x32_bf16 v[132:135], a[36:39], v[76:79], v[132:135]\n"
    "ds_read_b64_tr_b16 a[14:15], v7 offset:23040\n"
    "v_sub_u32 v220, v220, v203\n"
    "s_barrier\n"
    "v_mfma_f32_16x16x32_bf16 v[132:135], a[40:43], v[88:91], v[132:135]\n"
    "ds_read_b64_tr_b16 a[16:17], v4 offset:20480\n"
    "v_cmp_ne_u32 vcc, 0x100020, v220\n"
    "v_mfma_f32_16x16x32_bf16 v[132:135], a[44:47], v[92:95], v[132:135]\n"
    "ds_read_b64_tr_b16 a[18:19], v4 offset:20992\n"
    "s_and_b32 s55, vcc_lo, s67\n"
    "s_cbranch_scc1 .Lo_1_%=\n"
    ".Lr_2_%=:\n"
    "v_mfma_f32_16x16x32_bf16 v[128:131], a[40:43], v[80:83], v[128:131]\n"
    "ds_read_b64_tr_b16 a[20:21], v8 offset:20480\n"
    "v_mov_b32 v220, 0x200000\n"
    "v_mfma_f32_16x16x32_bf16 v[128:131], a[44:47], v[84:87], v[128:131]\n"
    "ds_read_b64_tr_b16 a[22:23], v8 offset:20992\n"
    "s_waitcnt lgkmcnt(4)\n"
    "v_mfma_f32_16x16x32_bf16 v[136:139], a[0:3], v[64:67], v[136:139]\n"
    "ds_read_b64_tr_b16 a[24:25], v4 offset:22528\n"
    "buffer_atomic_add v220, v218, s[8:11], 0 offen\n"
    "v_mfma_f32_16x16x32_bf16 v[136:139], a[4:7], v[68:71], v[136:139]\n"
    "ds_read_b64_tr_b16 a[26:27], v4 offset:23040\n"
    "v_cvt_pk_bf16_f32 v238, v188, v189\n"
    "v_cvt_pk_bf16_f32 v239, v190, v191\n"
    "v_mfma_f32_16x16x32_bf16 v[140:143], a[0:3], v[72:75], v[140:143]\n"
    "ds_read_b64_tr_b16 a[28:29], v8 offset:22528\n"
    "s_cmp_eq_u32 s89, 2\n"
    "s_cbranch_scc0 .Lr_6_%=\n"
    "s_mov_b32 exec_lo, 0\n"
    "s_mov_b32 exec_hi, 0xffff0000\n"
    "v_mov_b32 v228, 0x3f803f80\n"
    "v_mov_b32 v229, 0x3f80\n"
    "v_mov_b32 v236, 0x3f80\n"
    "v_mov_b32 v237, 0\n"
    "v_mov_b32 v230, 0x3f803f80\n"
    "v_mov_b32 v231, 0x3f80\n"
    "v_mov_b32 v238, 0x3f80\n"
    "v_mov_b32 v239, 0\n"
    "s_mov_b64 exec, -1\n"
    "v_swap_b32 v226, v228\n"
    "v_swap_b32 v227, v229\n"
    "ds_write_b64 v242, v[232:233] offset:36864\n"
    "ds_write_b64 v242, v[234:235] offset:38016\n"
    "v_mfma_f32_16x16x32_bf16 v[192:195], v[224:227], v[224:227], 0\n"
    "ds_write_b64 v242, v[236:237] offset:36896\n"
    "ds_write_b64 v242, v[238:239] offset:38048\n"
    "v_mfma_f32_16x16x32_bf16 v[196:199], v[228:231], v[228:231], 0\n"
    "ds_read_b64_tr_b16 v[232:233], v243 offset:36864\n"
    "ds_read_b64_tr_b16 v[234:235], v243 offset:37440\n"
    "ds_read_b64_tr_b16 v[236:237], v243 offset:38016\n"
    "ds_read_b64_tr_b16 v[238:239], v243 offset:38592\n"
    ".Lr_6_%=:\n"
    "v_mfma_f32_16x16x32_bf16 v[140:143], a[4:7], v[76:79], v[140:143]\n"
    "ds_read_b64_tr_b16 a[30:31], v8 offset:23040\n"
    "buffer_store_dwordx4 v[224:227], v241, s[8:11], s52 offen sc1\n"
    "v_mfma_f32_16x16x32_bf16 v[140:143], a[8:11], v[88:91], v[140:143]\n"
    "ds_read_b64_tr_b16 a[32:33], v5 offset:20480\n"
    "s_add_u32 m0, s78, 8192\n"
    "v_mfma_f32_16x16x32_bf16 v[140:143], a[12:15], v[92:95], v[140:143]\n"
    "ds_read_b64_tr_b16 a[34:35], v5 offset:20992\n"
    "buffer_store_dwordx4 v[228:231], v241, s[8:11], s53 offen offset:256 sc1\n"
    "v_mfma_f32_16x16x32_bf16 v[136:139], a[8:11], v[80:83], v[136:139]\n"
    "ds_read_b64_tr_b16 a[36:37], v9 offset:20480\n"
    "v_max3_f32 v245, v192, v193, v194\n"
    "v_mfma_f32_16x16x32_bf16 v[136:139], a[12:15], v[84:87], v[136:139]\n"
    "ds_read_b64_tr_b16 a[38:39], v9 offset:20992\n"
    "s_waitcnt lgkmcnt(4)\n"
    "v_mfma_f32_16x16x32_bf16 v[144:147], a[16:19], v[64:67], v[144:147]\n"
    "ds_read_b64_tr_b16 a[40:41], v5 offset:22528\n"
    "v_max3_f32 v246, v195, v196, v197\n"
    "v_mfma_f32_16x16x32_bf16 v[144:147], a[20:23], v[68:71], v[144:147]\n"
    "ds_read_b64_tr_b16 a[42:43], v5 offset:23040\n"
    "v_max3_f32 v245, v245, v198, v199\n"
    "v_mfma_f32_16x16x32_bf16 v[148:151], a[16:19], v[72:75], v[148:151]\n"
    "ds_read_b64_tr_b16 a[44:45], v9 offset:22528\n"
    "v_max3_f32 v245, v245, v246, 0\n"
    "v_mfma_f32_16x16x32_bf16 v[148:151], a[20:23], v[76:79], v[148:151]\n"
    "ds_read_b64_tr_b16 a[46:47], v9 offset:23040\n"
    "v_max_u32_dpp v245, v245, v245 row_ror:8 row_mask:0xf bank_mask:0xf\n"
    "v_mfma_f32_16x16x32_bf16 v[148:151], a[24:27], v[88:91], v[148:151]\n"
    "ds_read_b64_tr_b16 a[0:1], v2 offset:24576\n"
    "v_max_u32_dpp v245, v245, v245 row_ror:4 row_mask:0xf bank_mask:0xf\n"
    "v_mfma_f32_16x16x32_bf16 v[148:151], a[28:31], v[92:95], v[148:151]\n"
    "ds_read_b64_tr_b16 a[2:3], v2 offset:25088\n"
    "v_max_u32_dpp v245, v245, v245 row_ror:2 row_mask:0xf bank_mask:0xf\n"
    "v_mfma_f32_16x16x32_bf16 v[144:147], a[24:27], v[80:83], v[144:147]\n"
    "ds_read_b64_tr_b16 a[4:5], v6 offset:24576\n"
    "v_max_u32_dpp v245, v245, v245 row_ror:1 row_mask:0xf bank_mask:0xf\n"
    "v_mfma_f32_16x16x32_bf16 v[144:147], a[28:31], v[84:87], v[144:147]\n"
    "ds_read_b64_tr_b16 a[6:7], v6 offset:25088\n"
    "s_waitcnt lgkmcnt(4)\n"
    "v_mfma_f32_16x16x32_bf16 v[152:155], a[32:35], v[64:67], v[152:155]\n"
    "ds_read_b64_tr_b16 a[8:9], v2 offset:26624\n"
    "buffer_store_dwordx4 v[232:235], v254, s[8:11], s52 offen sc1\n"
    "v_mfma_f32_16x16x32_bf16 v[152:155], a[36:39], v[68:71], v[152:155]\n"
    "ds_read_b64_tr_b16 a[10:11], v2 offset:27136\n"
    "s_mul_i32 s83, s91, 3584\n"
    "v_mfma_f32_16x16x32_bf16 v[156:159], a[32:35], v[72:75], v[156:159]\n"
    "ds_read_b64_tr_b16 a[12:13], v6 offset:26624\n"
    "buffer_store_dwordx4 v[236:239], v254, s[8:11], s54 offen offset:1024 sc1\n"
    "v_mfma_f32_16x16x32_bf16 v[156:159], a[36:39], v[76:79], v[156:159]\n"
    "ds_read_b64_tr_b16 a[14:15], v6 offset:27136\n"
    "v_mfma_f32_16x16x32_bf16 v[156:159], a[40:43], v[88:91], v[156:159]\n"
    "ds_read_b64_tr_b16 a[16:17], v3 offset:24576\n"
    "buffer_store_dword v245, v244, s[8:11], s59 offen sc1\n"
    "v_mfma_f32_16x16x32_bf16 v[156:159], a[44:47], v[92:95], v[156:159]\n"
    "ds_read_b64_tr_b16 a[18:19], v3 offset:25088\n"
    "s_lshl_b32 s81, s75, 6\n"
    "v_mfma_f32_16x16x32_bf16 v[152:155], a[40:43], v[80:83], v[152:155]\n"
    "ds_read_b64_tr_b16 a[20:21], v7 offset:24576\n"
    "s_lshl1_add_u32 s76, s76, s81\n"
    "v_mfma_f32_16x16x32_bf16 v[152:155], a[44:47], v[84:87], v[152:155]\n"
    "ds_read_b64_tr_b16 a[22:23], v7 offset:25088\n"
    "s_waitcnt lgkmcnt(4)\n"
    "v_mfma_f32_16x16x32_bf16 v[128:131], a[0:3], v[96:99], v[128:131]\n"
    "ds_read_b64_tr_b16 a[24:25], v3 offset:26624\n"
    "buffer_load_dwordx4 v14, s[12:15], s83 offen lds\n"
    "v_mfma_f32_16x16x32_bf16 v[128:131], a[4:7], v[100:103], v[128:131]\n"
    "ds_read_b64_tr_b16 a[26:27], v3 offset:27136\n"
    "s_lshl_b32 s76, s76, 10\n"
    "v_mfma_f32_16x16x32_bf16 v[132:135], a[0:3], v[104:107], v[132:135]\n"
    "ds_read_b64_tr_b16 a[28:29], v7 offset:26624\n"
    "s_add_u32 s76, s76, 5242880\n"
    "v_mfma_f32_16x16x32_bf16 v[132:135], a[4:7], v[108:111], v[132:135]\n"
    "ds_read_b64_tr_b16 a[30:31], v7 offset:27136\n"
    "buffer_load_dwordx4 v15, s[12:15], s83 offen offset:1024 lds\n"
    "v_mfma_f32_16x16x32_bf16 v[132:135], a[8:11], v[120:123], v[132:135]\n"
    "ds_read_b64_tr_b16 a[32:33], v4 offset:24576\n"
    "s_add_u32 m0, s78, 10240\n"
    "v_mfma_f32_16x16x32_bf16 v[132:135], a[12:15], v[124:127], v[132:135]\n"
    "ds_read_b64_tr_b16 a[34:35], v4 offset:25088\n"
    "s_mul_i32 s83, s92, 3584\n"
    "v_mfma_f32_16x16x32_bf16 v[128:131], a[8:11], v[112:115], v[128:131]\n"
    "ds_read_b64_tr_b16 a[36:37], v8 offset:24576\n"
    "buffer_load_dwordx4 v14, s[12:15], s83 offen lds\n"
    "v_mfma_f32_16x16x32_bf16 v[128:131], a[12:15], v[116:119], v[128:131]\n"
    "ds_read_b64_tr_b16 a[38:39], v8 offset:25088\n"
    "s_waitcnt lgkmcnt(4)\n"
    "v_mfma_f32_16x16x32_bf16 v[136:139], a[16:19], v[96:99], v[136:139]\n"
    "ds_read_b64_tr_b16 a[40:41], v4 offset:26624\n"
    "s_sub_u32 s75, s76, 2048\n"
    "v_mfma_f32_16x16x32_bf16 v[136:139], a[20:23], v[100:103], v[136:139]\n"
    "ds_read_b64_tr_b16 a[42:43], v4 offset:27136\n"
    "buffer_load_dwordx4 v15, s[12:15], s83 offen offset:1024 lds\n"
    "v_mfma_f32_16x16x32_bf16 v[140:143], a[16:19], v[104:107], v[140:143]\n"
    "ds_read_b64_tr_b16 a[44:45], v8 offset:26624\n"
    "s_xor_b32 s74, s75, 65536\n"
    "v_mfma_f32_16x16x32_bf16 v[140:143], a[20:23], v[108:111], v[140:143]\n"
    "ds_read_b64_tr_b16 a[46:47], v8 offset:27136\n"
    "v_add_u32 v221, 487936, v221\n"
    "v_mfma_f32_16x16x32_bf16 v[140:143], a[24:27], v[120:123], v[140:143]\n"
    "ds_read_b64_tr_b16 a[0:1], v5 offset:24576\n"
    "s_lshl_b32 s97, s68, 16\n"
    "v_mfma_f32_16x16x32_bf16 v[140:143], a[28:31], v[124:127], v[140:143]\n"
    "ds_read_b64_tr_b16 a[2:3], v5 offset:25088\n"
    "s_add_u32 s97, s97, 131072\n"
    "v_mfma_f32_16x16x32_bf16 v[136:139], a[24:27], v[112:115], v[136:139]\n"
    "ds_read_b64_tr_b16 a[4:5], v9 offset:24576\n"
    "v_add_u32 v195, s97, v1\n"
    "v_mfma_f32_16x16x32_bf16 v[136:139], a[28:31], v[116:119], v[136:139]\n"
    "ds_read_b64_tr_b16 a[6:7], v9 offset:25088\n"
    "s_waitcnt lgkmcnt(4)\n"
    "v_mfma_f32_16x16x32_bf16 v[144:147], a[32:35], v[96:99], v[144:147]\n"
    "ds_read_b64_tr_b16 a[8:9], v5 offset:26624\n"
    "s_add_u32 m0, s78, 12288\n"
    "v_mfma_f32_16x16x32_bf16 v[144:147], a[36:39], v[100:103], v[144:147]\n"
    "ds_read_b64_tr_b16 a[10:11], v5 offset:27136\n"
    "s_mul_i32 s83, s93, 3584\n"
    "v_mfma_f32_16x16x32_bf16 v[148:151], a[32:35], v[104:107], v[148:151]\n"
    "ds_read_b64_tr_b16 a[12:13], v9 offset:26624\n"
    "buffer_load_dwordx4 v14, s[12:15], s83 offen lds\n"
    "v_mfma_f32_16x16x32_bf16 v[148:151], a[36:39], v[108:111], v[148:151]\n"
    "ds_read_b64_tr_b16 a[14:15], v9 offset:27136\n"
    "s_and_b32 s99, s70, 15\n"
    "v_mfma_f32_16x16x32_bf16 v[148:151], a[40:43], v[120:123], v[148:151]\n"
    "ds_read_b64_tr_b16 a[16:17], v2 offset:28672\n"
    "s_lshl_b32 s99, s99, 12\n"
    "v_mfma_f32_16x16x32_bf16 v[148:151], a[44:47], v[124:127], v[148:151]\n"
    "ds_read_b64_tr_b16 a[18:19], v2 offset:29184\n"
    "buffer_load_dwordx4 v15, s[12:15], s83 offen offset:1024 lds\n"
    "v_mfma_f32_16x16x32_bf16 v[144:147], a[40:43], v[112:115], v[144:147]\n"
    "ds_read_b64_tr_b16 a[20:21], v6 offset:28672\n"
    "s_lshl4_add_u32 s82, s85, s86\n"
    "v_mfma_f32_16x16x32_bf16 v[144:147], a[44:47], v[116:119], v[144:147]\n"
    "ds_read_b64_tr_b16 a[22:23], v6 offset:29184\n"
    "s_waitcnt lgkmcnt(4)\n"
    "v_mfma_f32_16x16x32_bf16 v[152:155], a[0:3], v[96:99], v[152:155]\n"
    "ds_read_b64_tr_b16 a[24:25], v2 offset:30720\n"
    "buffer_load_dword v220, v216, s[8:11], s84 offen sc1\n"
    "v_mfma_f32_16x16x32_bf16 v[152:155], a[4:7], v[100:103], v[152:155]\n"
    "ds_read_b64_tr_b16 a[26:27], v2 offset:31232\n"
    "s_mul_i32 s82, s82, 7168\n"
    "v_mfma_f32_16x16x32_bf16 v[156:159], a[0:3], v[104:107], v[156:159]\n"
    "ds_read_b64_tr_b16 a[28:29], v6 offset:30720\n"
    "s_waitcnt vmcnt(7)\n"
    "v_mfma_f32_16x16x32_bf16 v[156:159], a[4:7], v[108:111], v[156:159]\n"
    "ds_read_b64_tr_b16 a[30:31], v6 offset:31232\n"
    "s_add_u32 s82, s82, 524288\n"
    "s_barrier\n"
    "v_mfma_f32_16x16x32_bf16 v[156:159], a[8:11], v[120:123], v[156:159]\n"
    "ds_read_b64_tr_b16 a[32:33], v3 offset:28672\n"
    "buffer_store_dword v219, v247, s[8:11], s66 offen sc1\n"
    "v_mfma_f32_16x16x32_bf16 v[156:159], a[12:15], v[124:127], v[156:159]\n"
    "ds_read_b64_tr_b16 a[34:35], v3 offset:29184\n"
    "s_add_u32 m0, s78, 14336\n"
    "v_mfma_f32_16x16x32_bf16 v[152:155], a[8:11], v[112:115], v[152:155]\n"
    "ds_read_b64_tr_b16 a[36:37], v7 offset:28672\n"
    "s_mul_i32 s83, s94, 3584\n"
    "v_mfma_f32_16x16x32_bf16 v[152:155], a[12:15], v[116:119], v[152:155]\n"
    "ds_read_b64_tr_b16 a[38:39], v7 offset:29184\n"
    "s_waitcnt lgkmcnt(4)\n"
    "s_cmp_eq_u32 s89, 0\n"
    "s_cbranch_scc1 .Lvn_8_%=\n"
    "v_pk_mul_f32 v[128:129], v[128:129], v[248:249] op_sel_hi:[1,0]\n"
    "v_pk_mul_f32 v[130:131], v[130:131], v[248:249] op_sel_hi:[1,0]\n"
    "v_pk_mul_f32 v[132:133], v[132:133], v[250:251] op_sel_hi:[1,0]\n"
    "v_pk_mul_f32 v[134:135], v[134:135], v[250:251] op_sel_hi:[1,0]\n"
    "v_cvt_pk_bf16_f32 v128, v128, v129\n"
    "v_cvt_pk_bf16_f32 v129, v130, v131\n"
    "v_cvt_pk_bf16_f32 v130, v132, v133\n"
    "v_cvt_pk_bf16_f32 v131, v134, v135\n"
    "v_pk_mul_f32 v[136:137], v[136:137], v[248:249] op_sel_hi:[1,0]\n"
    "v_pk_mul_f32 v[138:139], v[138:139], v[248:249] op_sel_hi:[1,0]\n"
    "v_pk_mul_f32 v[140:141], v[140:141], v[250:251] op_sel_hi:[1,0]\n"
    "v_pk_mul_f32 v[142:143], v[142:143], v[250:251] op_sel_hi:[1,0]\n"
    "v_cvt_pk_bf16_f32 v136, v136, v137\n"
    "v_cvt_pk_bf16_f32 v137, v138, v139\n"
    "v_cvt_pk_bf16_f32 v138, v140, v141\n"
    "v_cvt_pk_bf16_f32 v139, v142, v143\n"
    "v_pk_mul_f32 v[144:145], v[144:145], v[248:249] op_sel_hi:[1,0]\n"
    "v_pk_mul_f32 v[146:147], v[146:147], v[248:249] op_sel_hi:[1,0]\n"
    "v_pk_mul_f32 v[148:149], v[148:149], v[250:251] op_sel_hi:[1,0]\n"
    "v_pk_mul_f32 v[150:151], v[150:151], v[250:251] op_sel_hi:[1,0]\n"
    "v_cvt_pk_bf16_f32 v144, v144, v145\n"
    "v_cvt_pk_bf16_f32 v145, v146, v147\n"
    "v_cvt_pk_bf16_f32 v146, v148, v149\n"
    "v_cvt_pk_bf16_f32 v147, v150, v151\n"
    "v_pk_mul_f32 v[152:153], v[152:153], v[248:249] op_sel_hi:[1,0]\n"
    "v_pk_mul_f32 v[154:155], v[154:155], v[248:249] op_sel_hi:[1,0]\n"
    "v_pk_mul_f32 v[156:157], v[156:157], v[250:251] op_sel_hi:[1,0]\n"
    "v_pk_mul_f32 v[158:159], v[158:159], v[250:251] op_sel_hi:[1,0]\n"
    "v_cvt_pk_bf16_f32 v152, v152, v153\n"
    "v_cvt_pk_bf16_f32 v153, v154, v155\n"
    "v_cvt_pk_bf16_f32 v154, v156, v157\n"
    "v_cvt_pk_bf16_f32 v155, v158, v159\n"
    "s_branch .Lve_7_%=\n"
    ".Lvn_8_%=:\n"
    "v_pk_mul_f32 v[160:161], v[128:129], v[160:161]\n"
    "v_pk_mul_f32 v[162:163], v[130:131], v[162:163]\n"
    "v_pk_mul_f32 v[168:169], v[136:137], v[168:169]\n"
    "v_pk_mul_f32 v[170:171], v[138:139], v[170:171]\n"
    "v_cvt_pk_bf16_f32 v128, v160, v161\n"
    "v_cvt_pk_bf16_f32 v129, v162, v163\n"
    "v_cvt_pk_bf16_f32 v130, v168, v169\n"
    "v_cvt_pk_bf16_f32 v131, v170, v171\n"
    "v_pk_mul_f32 v[164:165], v[132:133], v[164:165]\n"
    "v_pk_mul_f32 v[166:167], v[134:135], v[166:167]\n"
    "v_pk_mul_f32 v[172:173], v[140:141], v[172:173]\n"
    "v_pk_mul_f32 v[174:175], v[142:143], v[174:175]\n"
    "v_cvt_pk_bf16_f32 v132, v164, v165\n"
    "v_cvt_pk_bf16_f32 v133, v166, v167\n"
    "v_cvt_pk_bf16_f32 v134, v172, v173\n"
    "v_cvt_pk_bf16_f32 v135, v174, v175\n"
    "v_pk_mul_f32 v[176:177], v[144:145], v[176:177]\n"
    "v_pk_mul_f32 v[178:179], v[146:147], v[178:179]\n"
    "v_pk_mul_f32 v[184:185], v[152:153], v[184:185]\n"
    "v_pk_mul_f32 v[186:187], v[154:155], v[186:187]\n"
    "v_cvt_pk_bf16_f32 v144, v176, v177\n"
    "v_cvt_pk_bf16_f32 v145, v178, v179\n"
    "v_cvt_pk_bf16_f32 v146, v184, v185\n"
    "v_cvt_pk_bf16_f32 v147, v186, v187\n"
    "v_pk_mul_f32 v[180:181], v[148:149], v[180:181]\n"
    "v_pk_mul_f32 v[182:183], v[150:151], v[182:183]\n"
    "v_pk_mul_f32 v[188:189], v[156:157], v[188:189]\n"
    "v_pk_mul_f32 v[190:191], v[158:159], v[190:191]\n"
    "v_cvt_pk_bf16_f32 v148, v180, v181\n"
    "v_cvt_pk_bf16_f32 v149, v182, v183\n"
    "v_cvt_pk_bf16_f32 v150, v188, v189\n"
    "v_cvt_pk_bf16_f32 v151, v190, v191\n"
    ".Lvn_9_%=:\n"
    ".Lve_7_%=:\n"
    "v_mfma_f32_16x16x32_bf16 v[160:163], a[16:19], v[16:19], 0\n"
    "ds_read_b64_tr_b16 a[40:41], v3 offset:30720\n"
    "buffer_store_dwordx4 v[128:131], v240, s[8:11], s62 offen sc1\n"
    "v_mfma_f32_16x16x32_bf16 v[160:163], a[20:23], v[20:23], v[160:163]\n"
    "ds_read_b64_tr_b16 a[42:43], v3 offset:31232\n"
    "s_add_u32 s62, s62, 4096\n"
    "v_mfma_f32_16x16x32_bf16 v[164:167], a[16:19], v[24:27], 0\n"
    "ds_read_b64_tr_b16 a[44:45], v7 offset:30720\n"
    "s_bitcmp0_b32 s68, 0\n"
    "s_cselect_b32 s64, 4096, 0\n"
    "v_mfma_f32_16x16x32_bf16 v[164:167], a[20:23], v[28:31], v[164:167]\n"
    "ds_read_b64_tr_b16 a[46:47], v7 offset:31232\n"
    "s_add_u32 s82, s82, s64\n"
    "v_mfma_f32_16x16x32_bf16 v[164:167], a[24:27], v[40:43], v[164:167]\n"
    "ds_read_b64_tr_b16 a[0:1], v4 offset:28672\n"
    "buffer_store_dwordx4 v[136:139], v240, s[8:11], s62 offen sc1\n"
    "v_mfma_f32_16x16x32_bf16 v[164:167], a[28:31], v[44:47], v[164:167]\n"
    "ds_read_b64_tr_b16 a[2:3], v4 offset:29184\n"
    "s_add_u32 s62, s62, 4096\n"
    "v_mfma_f32_16x16x32_bf16 v[160:163], a[24:27], v[32:35], v[160:163]\n"
    "ds_read_b64_tr_b16 a[4:5], v8 offset:28672\n"
    "s_and_b32 s65, s68, 2\n"
    "v_mfma_f32_16x16x32_bf16 v[160:163], a[28:31], v[36:39], v[160:163]\n"
    "ds_read_b64_tr_b16 a[6:7], v8 offset:29184\n"
    "s_waitcnt lgkmcnt(4)\n"
    "v_mfma_f32_16x16x32_bf16 v[168:171], a[32:35], v[16:19], 0\n"
    "ds_read_b64_tr_b16 a[8:9], v4 offset:30720\n"
    "buffer_store_dwordx4 v[144:147], v240, s[8:11], s62 offen sc1\n"
    "v_mfma_f32_16x16x32_bf16 v[168:171], a[36:39], v[20:23], v[168:171]\n"
    "ds_read_b64_tr_b16 a[10:11], v4 offset:31232\n"
    "s_add_u32 s62, s62, 4096\n"
    "v_mfma_f32_16x16x32_bf16 v[172:175], a[32:35], v[24:27], 0\n"
    "ds_read_b64_tr_b16 a[12:13], v8 offset:30720\n"
    "s_lshl_b32 s65, s65, 8\n"
    "v_mfma_f32_16x16x32_bf16 v[172:175], a[36:39], v[28:31], v[172:175]\n"
    "ds_read_b64_tr_b16 a[14:15], v8 offset:31232\n"
    "s_xor_b32 s64, s65, 512\n"
    "v_mfma_f32_16x16x32_bf16 v[172:175], a[40:43], v[40:43], v[172:175]\n"
    "ds_read_b64_tr_b16 a[16:17], v5 offset:28672\n"
    "buffer_store_dwordx4 v[152:155], v240, s[8:11], s62 offen sc1\n"
    "v_mfma_f32_16x16x32_bf16 v[172:175], a[44:47], v[44:47], v[172:175]\n"
    "ds_read_b64_tr_b16 a[18:19], v5 offset:29184\n"
    "s_add_u32 s64, s64, s82\n"
    "v_mfma_f32_16x16x32_bf16 v[168:171], a[40:43], v[32:35], v[168:171]\n"
    "ds_read_b64_tr_b16 a[20:21], v9 offset:28672\n"
    "s_add_u32 s65, s65, s82\n"
    "v_mfma_f32_16x16x32_bf16 v[168:171], a[44:47], v[36:39], v[168:171]\n"
    "ds_read_b64_tr_b16 a[22:23], v9 offset:29184\n"
    "s_waitcnt lgkmcnt(4)\n"
    "v_mfma_f32_16x16x32_bf16 v[176:179], a[0:3], v[16:19], 0\n"
    "ds_read_b64_tr_b16 a[24:25], v5 offset:30720\n"
    "buffer_load_dwordx4 v14, s[12:15], s83 offen lds\n"
    "v_mfma_f32_16x16x32_bf16 v[176:179], a[4:7], v[20:23], v[176:179]\n"
    "ds_read_b64_tr_b16 a[26:27], v5 offset:31232\n"
    "s_and_b32 s67, s85, 3\n"
    "v_mfma_f32_16x16x32_bf16 v[180:183], a[0:3], v[24:27], 0\n"
    "ds_read_b64_tr_b16 a[28:29], v9 offset:30720\n"
    "s_lshr_b32 s82, s85, 2\n"
    "v_mfma_f32_16x16x32_bf16 v[180:183], a[4:7], v[28:31], v[180:183]\n"
    "ds_read_b64_tr_b16 a[30:31], v9 offset:31232\n"
    "buffer_load_dwordx4 v15, s[12:15], s83 offen offset:1024 lds\n"
    "v_mfma_f32_16x16x32_bf16 v[180:183], a[8:11], v[40:43], v[180:183]\n"
    "ds_read_b64_tr_b16 a[32:33], v2 offset:32768\n"
    "s_add_u32 m0, s78, 16384\n"
    "v_mfma_f32_16x16x32_bf16 v[180:183], a[12:15], v[44:47], v[180:183]\n"
    "ds_read_b64_tr_b16 a[34:35], v2 offset:33280\n"
    "s_mul_i32 s83, s95, 3584\n"
    "v_mfma_f32_16x16x32_bf16 v[176:179], a[8:11], v[32:35], v[176:179]\n"
    "ds_read_b64_tr_b16 a[36:37], v6 offset:32768\n"
    "buffer_load_dwordx4 v14, s[12:15], s83 offen lds\n"
    "v_mfma_f32_16x16x32_bf16 v[176:179], a[12:15], v[36:39], v[176:179]\n"
    "ds_read_b64_tr_b16 a[38:39], v6 offset:33280\n"
    "s_waitcnt lgkmcnt(4)\n"
    "v_mfma_f32_16x16x32_bf16 v[184:187], a[16:19], v[16:19], 0\n"
    "ds_read_b64_tr_b16 a[40:41], v2 offset:34816\n"
    "s_lshl3_add_u32 s67, s67, s82\n"
    "v_mfma_f32_16x16x32_bf16 v[184:187], a[20:23], v[20:23], v[184:187]\n"
    "ds_read_b64_tr_b16 a[42:43], v2 offset:35328\n"
    "buffer_load_dwordx4 v15, s[12:15], s83 offen offset:1024 lds\n"
    "v_mfma_f32_16x16x32_bf16 v[188:191], a[16:19], v[24:27], 0\n"
    "ds_read_b64_tr_b16 a[44:45], v6 offset:34816\n"
    "s_lshl_b32 s67, s67, 7\n"
    "v_mfma_f32_16x16x32_bf16 v[188:191], a[20:23], v[28:31], v[188:191]\n"
    "ds_read_b64_tr_b16 a[46:47], v6 offset:35328\n"
    "s_add_u32 s67, s67, 438272\n"
    "v_mfma_f32_16x16x32_bf16 v[188:191], a[24:27], v[40:43], v[188:191]\n"
    "ds_read_b64_tr_b16 a[0:1], v3 offset:32768\n"
    "v_and_b32 v192, 1, v0\n"
    "v_mfma_f32_16x16x32_bf16 v[188:191], a[28:31], v[44:47], v[188:191]\n"
    "ds_read_b64_tr_b16 a[2:3], v3 offset:33280\n"
    "v_and_b32 v193, 2, v0\n"
    "v_mfma_f32_16x16x32_bf16 v[184:187], a[24:27], v[32:35], v[184:187]\n"
    "ds_read_b64_tr_b16 a[4:5], v7 offset:32768\n"
    "v_lshlrev_b32 v192, 14, v192\n"
    "v_mfma_f32_16x16x32_bf16 v[184:187], a[28:31], v[36:39], v[184:187]\n"
    "ds_read_b64_tr_b16 a[6:7], v7 offset:33280\n"
    "s_waitcnt lgkmcnt(4)\n"
    "v_mfma_f32_16x16x32_bf16 v[160:163], a[32:35], v[48:51], v[160:163]\n"
    "ds_read_b64_tr_b16 a[8:9], v3 offset:34816\n"
    "s_waitcnt vmcnt(9)\n"
    "v_mfma_f32_16x16x32_bf16 v[160:163], a[36:39], v[52:55], v[160:163]\n"
    "ds_read_b64_tr_b16 a[10:11], v3 offset:35328\n"
    "v_sub_u32 v220, v220, v201\n"
    "v_mfma_f32_16x16x32_bf16 v[164:167], a[32:35], v[56:59], v[164:167]\n"
    "ds_read_b64_tr_b16 a[12:13], v7 offset:34816\n"
    "v_cmp_gt_u32 vcc, 0x200000, v220\n"
    "v_mfma_f32_16x16x32_bf16 v[164:167], a[36:39], v[60:63], v[164:167]\n"
    "ds_read_b64_tr_b16 a[14:15], v7 offset:35328\n"
    "s_and_b32 s55, vcc_lo, s58\n"
    "s_cbranch_scc1 .Lo_10_%=\n"
    ".Lr_11_%=:\n"
    "v_mfma_f32_16x16x32_bf16 v[164:167], a[40:43], v[72:75], v[164:167]\n"
    "ds_read_b64_tr_b16 a[16:17], v4 offset:32768\n"
    "s_add_u32 m0, s78, 18432\n"
    "v_mfma_f32_16x16x32_bf16 v[164:167], a[44:47], v[76:79], v[164:167]\n"
    "ds_read_b64_tr_b16 a[18:19], v4 offset:33280\n"
    "s_mul_i32 s83, s96, 3584\n"
    "v_mfma_f32_16x16x32_bf16 v[160:163], a[40:43], v[64:67], v[160:163]\n"
    "ds_read_b64_tr_b16 a[20:21], v8 offset:32768\n"
    "buffer_load_dwordx4 v14, s[12:15], s83 offen lds\n"
    "v_mfma_f32_16x16x32_bf16 v[160:163], a[44:47], v[68:71], v[160:163]\n"
    "ds_read_b64_tr_b16 a[22:23], v8 offset:33280\n"
    "s_waitcnt lgkmcnt(4)\n"
    "v_mfma_f32_16x16x32_bf16 v[168:171], a[0:3], v[48:51], v[168:171]\n"
    "ds_read_b64_tr_b16 a[24:25], v4 offset:34816\n"
    "v_lshl_add_u32 v218, v193, 8, v192\n"
    "v_mfma_f32_16x16x32_bf16 v[168:171], a[4:7], v[52:55], v[168:171]\n"
    "ds_read_b64_tr_b16 a[26:27], v4 offset:35328\n"
    "buffer_load_dwordx4 v15, s[12:15], s83 offen offset:1024 lds\n"
    "v_mfma_f32_16x16x32_bf16 v[172:175], a[0:3], v[56:59], v[172:175]\n"
    "ds_read_b64_tr_b16 a[28:29], v8 offset:34816\n"
    "s_lshr_b32 s54, s87, 1\n"
    "v_mfma_f32_16x16x32_bf16 v[172:175], a[4:7], v[60:63], v[172:175]\n"
    "ds_read_b64_tr_b16 a[30:31], v8 offset:35328\n"
    "s_and_b32 s53, s54, 3\n"
    "v_mfma_f32_16x16x32_bf16 v[172:175], a[8:11], v[72:75], v[172:175]\n"
    "ds_read_b64_tr_b16 a[32:33], v5 offset:32768\n"
    "s_lshr_b32 s82, s54, 2\n"
    "v_mfma_f32_16x16x32_bf16 v[172:175], a[12:15], v[76:79], v[172:175]\n"
    "ds_read_b64_tr_b16 a[34:35], v5 offset:33280\n"
    "s_lshl3_add_u32 s53, s53, s82\n"
    "v_mfma_f32_16x16x32_bf16 v[168:171], a[8:11], v[64:67], v[168:171]\n"
    "ds_read_b64_tr_b16 a[36:37], v9 offset:32768\n"
    "s_lshl_b32 s53, s53, 7\n"
    "v_mfma_f32_16x16x32_bf16 v[168:171], a[12:15], v[68:71], v[168:171]\n"
    "ds_read_b64_tr_b16 a[38:39], v9 offset:33280\n"
    "s_waitcnt lgkmcnt(4)\n"
    "v_mfma_f32_16x16x32_bf16 v[176:179], a[16:19], v[48:51], v[176:179]\n"
    "ds_read_b64_tr_b16 a[40:41], v5 offset:34816\n"
    "s_add_u32 s53, s53, 393216\n"
    "v_mfma_f32_16x16x32_bf16 v[176:179], a[20:23], v[52:55], v[176:179]\n"
    "ds_read_b64_tr_b16 a[42:43], v5 offset:35328\n"
    "s_xor_b32 s82, s68, 3\n"
    "v_mfma_f32_16x16x32_bf16 v[180:183], a[16:19], v[56:59], v[180:183]\n"
    "ds_read_b64_tr_b16 a[44:45], v9 offset:34816\n"
    "s_waitcnt vmcnt(6)\n"
    "v_mfma_f32_16x16x32_bf16 v[180:183], a[20:23], v[60:63], v[180:183]\n"
    "ds_read_b64_tr_b16 a[46:47], v9 offset:35328\n"
    "s_lshl2_add_u32 s61, s87, s82\n"
    "s_barrier\n"
    "v_mfma_f32_16x16x32_bf16 v[180:183], a[24:27], v[72:75], v[180:183]\n"
    "ds_read_b64_tr_b16 a[0:1], v2\n"
    "buffer_store_dword v219, v217, s[8:11], s66 offen sc1\n"
    "v_mfma_f32_16x16x32_bf16 v[180:183], a[28:31], v[76:79], v[180:183]\n"
    "ds_read_b64_tr_b16 a[2:3], v2 offset:512\n"
    "s_lshl_b32 s60, s61, 16\n"
    "v_mfma_f32_16x16x32_bf16 v[176:179], a[24:27], v[64:67], v[176:179]\n"
    "ds_read_b64_tr_b16 a[4:5], v6\n"
    "s_mul_i32 s82, s68, 14336\n"
    "v_mfma_f32_16x16x32_bf16 v[176:179], a[28:31], v[68:71], v[176:179]\n"
    "ds_read_b64_tr_b16 a[6:7], v6 offset:512\n"
    "s_waitcnt lgkmcnt(4)\n"
    "v_mfma_f32_16x16x32_bf16 v[184:187], a[32:35], v[48:51], v[184:187]\n"
    "ds_read_b64_tr_b16 a[8:9], v2 offset:2048\n"
    "s_add_u32 s60, s60, s82\n"
    "v_mfma_f32_16x16x32_bf16 v[184:187], a[36:39], v[52:55], v[184:187]\n"
    "ds_read_b64_tr_b16 a[10:11], v2 offset:2560\n"
    "s_add_u32 s60, s60, 5242880\n"
    "v_mfma_f32_16x16x32_bf16 v[188:191], a[32:35], v[56:59], v[188:191]\n"
    "ds_read_b64_tr_b16 a[12:13], v6 offset:2048\n"
    "s_lshl2_add_u32 s61, s61, s68\n"
    "v_mfma_f32_16x16x32_bf16 v[188:191], a[36:39], v[60:63], v[188:191]\n"
    "ds_read_b64_tr_b16 a[14:15], v6 offset:2560\n"
    "v_xor_b32 v209, 32, v208\n"
    "v_mfma_f32_16x16x32_bf16 v[188:191], a[40:43], v[72:75], v[188:191]\n"
    "ds_read_b64_tr_b16 a[16:17], v3\n"
    "buffer_load_dword v220, v221, s[8:11], 0 offen sc1\n"
    "v_mfma_f32_16x16x32_bf16 v[188:191], a[44:47], v[76:79], v[188:191]\n"
    "ds_read_b64_tr_b16 a[18:19], v3 offset:512\n"
    "v_xor_b32 v210, 64, v208\n"
    "v_mfma_f32_16x16x32_bf16 v[184:187], a[40:43], v[64:67], v[184:187]\n"
    "ds_read_b64_tr_b16 a[20:21], v7\n"
    "v_xor_b32 v211, 96, v208\n"
    "v_mfma_f32_16x16x32_bf16 v[184:187], a[44:47], v[68:71], v[184:187]\n"
    "ds_read_b64_tr_b16 a[22:23], v7 offset:512\n"
    "s_waitcnt lgkmcnt(4)\n"
    "v_mfma_f32_16x16x32_bf16 v[160:163], a[0:3], v[80:83], v[160:163]\n"
    "ds_read_b64_tr_b16 a[24:25], v3 offset:2048\n"
    "v_add_u32 v10, s60, v1\n"
    "v_mfma_f32_16x16x32_bf16 v[160:163], a[4:7], v[84:87], v[160:163]\n"
    "ds_read_b64_tr_b16 a[26:27], v3 offset:2560\n"
    "v_xor_b32 v11, 65536, v10\n"
    "v_mfma_f32_16x16x32_bf16 v[164:167], a[0:3], v[88:91], v[164:167]\n"
    "ds_read_b64_tr_b16 a[28:29], v7 offset:2048\n"
    "v_xor_b32 v12, 131072, v10\n"
    "v_mfma_f32_16x16x32_bf16 v[164:167], a[4:7], v[92:95], v[164:167]\n"
    "ds_read_b64_tr_b16 a[30:31], v7 offset:2560\n"
    "v_xor_b32 v13, 196608, v10\n"
    "v_mfma_f32_16x16x32_bf16 v[164:167], a[8:11], v[104:107], v[164:167]\n"
    "ds_read_b64_tr_b16 a[32:33], v4\n"
    "v_add_u32 v215, s78, v1\n"
    "v_mfma_f32_16x16x32_bf16 v[164:167], a[12:15], v[108:111], v[164:167]\n"
    "ds_read_b64_tr_b16 a[34:35], v4 offset:512\n"
    "s_xor_b32 s52, s68, 3\n"
    "v_mfma_f32_16x16x32_bf16 v[160:163], a[8:11], v[96:99], v[160:163]\n"
    "ds_read_b64_tr_b16 a[36:37], v8\n"
    "s_mul_i32 s52, s52, 40960\n"
    "v_mfma_f32_16x16x32_bf16 v[160:163], a[12:15], v[100:103], v[160:163]\n"
    "ds_read_b64_tr_b16 a[38:39], v8 offset:512\n"
    "s_waitcnt lgkmcnt(4)\n"
    "v_mfma_f32_16x16x32_bf16 v[168:171], a[16:19], v[80:83], v[168:171]\n"
    "ds_read_b64_tr_b16 a[40:41], v4 offset:2048\n"
    "buffer_store_dwordx4 v[128:131], v253, s[8:11], s75 offen sc1\n"
    "v_mfma_f32_16x16x32_bf16 v[168:171], a[20:23], v[84:87], v[168:171]\n"
    "ds_read_b64_tr_b16 a[42:43], v4 offset:2560\n"
    "v_add_u32 v212, s52, v1\n"
    "v_mfma_f32_16x16x32_bf16 v[172:175], a[16:19], v[88:91], v[172:175]\n"
    "ds_read_b64_tr_b16 a[44:45], v8 offset:2048\n"
    "s_xor_b32 s52, s68, 2\n"
    "v_mfma_f32_16x16x32_bf16 v[172:175], a[20:23], v[92:95], v[172:175]\n"
    "ds_read_b64_tr_b16 a[46:47], v8 offset:2560\n"
    "s_mul_i32 s52, s52, 40960\n"
    "v_mfma_f32_16x16x32_bf16 v[172:175], a[24:27], v[104:107], v[172:175]\n"
    "ds_read_b64_tr_b16 a[0:1], v5\n"
    "buffer_store_dwordx4 v[132:135], v253, s[8:11], s74 offen sc1\n"
    "v_mfma_f32_16x16x32_bf16 v[172:175], a[28:31], v[108:111], v[172:175]\n"
    "ds_read_b64_tr_b16 a[2:3], v5 offset:512\n"
    "v_add_u32 v213, s52, v1\n"
    "v_mfma_f32_16x16x32_bf16 v[168:171], a[24:27], v[96:99], v[168:171]\n"
    "ds_read_b64_tr_b16 a[4:5], v9\n"
    "s_xor_b32 s52, s68, 1\n"
    "v_mfma_f32_16x16x32_bf16 v[168:171], a[28:31], v[100:103], v[168:171]\n"
    "ds_read_b64_tr_b16 a[6:7], v9 offset:512\n"
    "s_waitcnt lgkmcnt(4)\n"
    "v_mfma_f32_16x16x32_bf16 v[176:179], a[32:35], v[80:83], v[176:179]\n"
    "ds_read_b64_tr_b16 a[8:9], v5 offset:2048\n"
    "buffer_store_dwordx4 v[144:147], v253, s[8:11], s75 offen offset:1024 sc1\n"
    "v_mfma_f32_16x16x32_bf16 v[176:179], a[36:39], v[84:87], v[176:179]\n"
    "ds_read_b64_tr_b16 a[10:11], v5 offset:2560\n"
    "s_mul_i32 s52, s52, 40960\n"
    "v_mfma_f32_16x16x32_bf16 v[180:183], a[32:35], v[88:91], v[180:183]\n"
    "ds_read_b64_tr_b16 a[12:13], v9 offset:2048\n"
    "v_add_u32 v214, s52, v1\n"
    "v_mfma_f32_16x16x32_bf16 v[180:183], a[36:39], v[92:95], v[180:183]\n"
    "ds_read_b64_tr_b16 a[14:15], v9 offset:2560\n"
    "s_mul_i32 s57, s68, 100352\n"
    "v_mfma_f32_16x16x32_bf16 v[180:183], a[40:43], v[104:107], v[180:183]\n"
    "ds_read_b64_tr_b16 a[16:17], v2 offset:4096\n"
    "buffer_store_dwordx4 v[148:151], v253, s[8:11], s74 offen offset:1024 sc1\n"
    "v_mfma_f32_16x16x32_bf16 v[180:183], a[44:47], v[108:111], v[180:183]\n"
    "ds_read_b64_tr_b16 a[18:19], v2 offset:4608\n"
    "s_cmp_lt_u32 s88, 7\n"
    "s_cselect_b32 s22, s22, 0\n"
    "s_cselect_b32 s26, s26, 0\n"
    "s_cselect_b32 s34, s34, 0\n"
    "v_mfma_f32_16x16x32_bf16 v[176:179], a[40:43], v[96:99], v[176:179]\n"
    "ds_read_b64_tr_b16 a[20:21], v6 offset:4096\n"
    "s_mul_i32 s36, s90, 896\n"
    "v_mfma_f32_16x16x32_bf16 v[176:179], a[44:47], v[100:103], v[176:179]\n"
    "ds_read_b64_tr_b16 a[22:23], v6 offset:4608\n"
    "s_waitcnt lgkmcnt(4)\n"
    "v_mfma_f32_16x16x32_bf16 v[184:187], a[0:3], v[80:83], v[184:187]\n"
    "ds_read_b64_tr_b16 a[24:25], v3 offset:4096\n"
    "s_lshl_b32 s43, s90, 5\n"
    "v_mfma_f32_16x16x32_bf16 v[184:187], a[4:7], v[84:87], v[184:187]\n"
    "ds_read_b64_tr_b16 a[26:27], v3 offset:4608\n"
    "s_mul_i32 s37, s91, 896\n"
    "v_mfma_f32_16x16x32_bf16 v[188:191], a[0:3], v[88:91], v[188:191]\n"
    ".Lpage_3_%=:\n"
    "s_cbranch_execz .Lpage_7_%=\n"
    "ds_read_b64_tr_b16 a[28:29], v7 offset:4096\n"
    "s_lshl_b32 s44, s91, 5\n"
    "v_mfma_f32_16x16x32_bf16 v[188:191], a[4:7], v[92:95], v[188:191]\n"
    "ds_read_b64_tr_b16 a[30:31], v7 offset:4608\n"
    "s_mul_i32 s38, s92, 896\n"
    "v_mfma_f32_16x16x32_bf16 v[188:191], a[8:11], v[104:107], v[188:191]\n"
    "ds_read_b64_tr_b16 a[32:33], v4 offset:4096\n"
    "s_lshl_b32 s45, s92, 5\n"
    "v_mfma_f32_16x16x32_bf16 v[188:191], a[12:15], v[108:111], v[188:191]\n"
    "ds_read_b64_tr_b16 a[34:35], v4 offset:4608\n"
    "s_mul_i32 s39, s93, 896\n"
    "v_mfma_f32_16x16x32_bf16 v[184:187], a[8:11], v[96:99], v[184:187]\n"
    "ds_read_b64_tr_b16 a[36:37], v8 offset:4096\n"
    "s_lshl_b32 s46, s93, 5\n"
    "v_mfma_f32_16x16x32_bf16 v[184:187], a[12:15], v[100:103], v[184:187]\n"
    "ds_read_b64_tr_b16 a[38:39], v8 offset:4608\n"
    "s_waitcnt lgkmcnt(4)\n"
    "v_mfma_f32_16x16x32_bf16 v[160:163], a[16:19], v[112:115], v[160:163]\n"
    "ds_read_b64_tr_b16 a[40:41], v5 offset:4096\n"
    "s_mul_i32 s40, s94, 896\n"
    "v_mfma_f32_16x16x32_bf16 v[160:163], a[20:23], v[116:119], v[160:163]\n"
    "ds_read_b64_tr_b16 a[42:43], v5 offset:4608\n"
    "s_lshl_b32 s47, s94, 5\n"
    "v_mfma_f32_16x16x32_bf16 v[164:167], a[16:19], v[120:123], v[164:167]\n"
    "ds_read_b64_tr_b16 a[44:45], v9 offset:4096\n"
    "s_mul_i32 s41, s95, 896\n"
    "v_mfma_f32_16x16x32_bf16 v[164:167], a[20:23], v[124:127], v[164:167]\n"
    "ds_read_b64_tr_b16 a[46:47], v9 offset:4608\n"
    "s_lshl_b32 s0, s95, 5\n"
    "v_mfma_f32_16x16x32_bf16 v[168:171], a[24:27], v[112:115], v[168:171]\n"
    "ds_read_b64_tr_b16 a[0:1], v2 offset:6144\n"
    "s_mul_i32 s42, s96, 896\n"
    "v_mfma_f32_16x16x32_bf16 v[168:171], a[28:31], v[116:119], v[168:171]\n"
    "ds_read_b64_tr_b16 a[2:3], v2 offset:6656\n"
    "s_lshl_b32 s1, s96, 5\n"
    "v_mfma_f32_16x16x32_bf16 v[172:175], a[24:27], v[120:123], v[172:175]\n"
    "ds_read_b64_tr_b16 a[4:5], v6 offset:6144\n"
    "s_lshl2_add_u32 s59, s87, s68\n"
    "v_mfma_f32_16x16x32_bf16 v[172:175], a[28:31], v[124:127], v[172:175]\n"
    "ds_read_b64_tr_b16 a[6:7], v6 offset:6656\n"
    "s_waitcnt lgkmcnt(4)\n"
    "v_mfma_f32_16x16x32_bf16 v[176:179], a[32:35], v[112:115], v[176:179]\n"
    "ds_read_b64_tr_b16 a[8:9], v3 offset:6144\n"
    "v_mul_f32 v160, v200, v160\n"
    "v_mfma_f32_16x16x32_bf16 v[176:179], a[36:39], v[116:119], v[176:179]\n"
    "ds_read_b64_tr_b16 a[10:11], v3 offset:6656\n"
    "v_mul_f32 v161, v200, v161\n"
    "v_mfma_f32_16x16x32_bf16 v[180:183], a[32:35], v[120:123], v[180:183]\n"
    "ds_read_b64_tr_b16 a[12:13], v7 offset:6144\n"
    "v_mul_f32 v162, v200, v162\n"
    "v_mfma_f32_16x16x32_bf16 v[180:183], a[36:39], v[124:127], v[180:183]\n"
    "ds_read_b64_tr_b16 a[14:15], v7 offset:6656\n"
    "v_mul_f32 v163, v200, v163\n"
    "v_mfma_f32_16x16x32_bf16 v[184:187], a[40:43], v[112:115], v[184:187]\n"
    "ds_read_b64_tr_b16 a[16:17], v4 offset:6144\n"
    "v_mul_f32 v164, v202, v164\n"
    "v_mfma_f32_16x16x32_bf16 v[184:187], a[44:47], v[116:119], v[184:187]\n"
    "ds_read_b64_tr_b16 a[18:19], v4 offset:6656\n"
    "v_mul_f32 v165, v202, v165\n"
    "v_mfma_f32_16x16x32_bf16 v[188:191], a[40:43], v[120:123], v[188:191]\n"
    "ds_read_b64_tr_b16 a[20:21], v8 offset:6144\n"
    "v_mul_f32 v166, v202, v166\n"
    "v_mfma_f32_16x16x32_bf16 v[188:191], a[44:47], v[124:127], v[188:191]\n"
    "ds_read_b64_tr_b16 a[22:23], v8 offset:6656\n"
    "s_waitcnt lgkmcnt(4)\n"
    "v_mfma_f32_16x16x32_bf16 v[128:131], a[0:3], v[16:19], 0\n"
    "ds_read_b64_tr_b16 a[24:25], v5 offset:6144\n"
    "s_waitcnt vmcnt(4)\n"
    "v_mfma_f32_16x16x32_bf16 v[128:131], a[4:7], v[20:23], v[128:131]\n"
    "ds_read_b64_tr_b16 a[26:27], v5 offset:6656\n"
    "v_cmp_ne_u32 vcc, 1, v220\n"
    "v_mfma_f32_16x16x32_bf16 v[132:135], a[0:3], v[24:27], 0\n"
    "ds_read_b64_tr_b16 a[28:29], v9 offset:6144\n"
    "s_cbranch_vccnz .Lo_15_%=\n"
    ".Lr_16_%=:\n"
    "v_mfma_f32_16x16x32_bf16 v[132:135], a[4:7], v[28:31], v[132:135]\n"
    "ds_read_b64_tr_b16 a[30:31], v9 offset:6656\n"
    "s_mov_b32 s72, 81920\n"
    "v_mfma_f32_16x16x32_bf16 v[136:139], a[8:11], v[16:19], 0\n"
    "ds_read_b64_tr_b16 a[32:33], v2 offset:8192\n"
    "buffer_load_dwordx4 v[196:199], v1, s[8:11], s72 offen sc1\n"
    "v_mfma_f32_16x16x32_bf16 v[136:139], a[12:15], v[20:23], v[136:139]\n"
    "ds_read_b64_tr_b16 a[34:35], v2 offset:8704\n"
    "s_xor_b32 s84, s99, 0\n"
    "v_mfma_f32_16x16x32_bf16 v[140:143], a[8:11], v[24:27], 0\n"
    "ds_read_b64_tr_b16 a[36:37], v6 offset:8192\n"
    "v_mul_f32 v167, v202, v167\n"
    "v_mfma_f32_16x16x32_bf16 v[140:143], a[12:15], v[28:31], v[140:143]\n"
    "ds_read_b64_tr_b16 a[38:39], v6 offset:8704\n"
    "s_waitcnt lgkmcnt(4)\n"
    "v_mfma_f32_16x16x32_bf16 v[144:147], a[16:19], v[16:19], 0\n"
    "ds_read_b64_tr_b16 a[40:41], v2 offset:10240\n"
    "v_mul_f32 v168, v200, v168\n"
    "v_mfma_f32_16x16x32_bf16 v[144:147], a[20:23], v[20:23], v[144:147]\n"
    "ds_read_b64_tr_b16 a[42:43], v2 offset:10752\n"
    "v_mul_f32 v169, v200, v169\n"
    "v_mfma_f32_16x16x32_bf16 v[148:151], a[16:19], v[24:27], 0\n"
    "ds_read_b64_tr_b16 a[44:45], v6 offset:10240\n"
    "v_mul_f32 v170, v200, v170\n"
    "v_mfma_f32_16x16x32_bf16 v[148:151], a[20:23], v[28:31], v[148:151]\n"
    "ds_read_b64_tr_b16 a[46:47], v6 offset:10752\n"
    "v_mul_f32 v171, v200, v171\n"
    "v_mfma_f32_16x16x32_bf16 v[152:155], a[24:27], v[16:19], 0\n"
    "ds_read_b64_tr_b16 a[0:1], v3 offset:8192\n"
    "v_mul_f32 v172, v202, v172\n"
    "v_mfma_f32_16x16x32_bf16 v[152:155], a[28:31], v[20:23], v[152:155]\n"
    "ds_read_b64_tr_b16 a[2:3], v3 offset:8704\n"
    "v_mul_f32 v173, v202, v173\n"
    "v_mfma_f32_16x16x32_bf16 v[156:159], a[24:27], v[24:27], 0\n"
    "ds_read_b64_tr_b16 a[4:5], v7 offset:8192\n"
    "v_mul_f32 v174, v202, v174\n"
    "v_mfma_f32_16x16x32_bf16 v[156:159], a[28:31], v[28:31], v[156:159]\n"
    "ds_read_b64_tr_b16 a[6:7], v7 offset:8704\n"
    "s_waitcnt lgkmcnt(4)\n"
    "v_mfma_f32_16x16x32_bf16 v[128:131], a[32:35], v[32:35], v[128:131]\n"
    "ds_read_b64_tr_b16 a[8:9], v3 offset:10240\n"
    "buffer_load_dword v220, v218, s[8:11], s67 offen sc1\n"
    "v_mfma_f32_16x16x32_bf16 v[128:131], a[36:39], v[36:39], v[128:131]\n"
    "ds_read_b64_tr_b16 a[10:11], v3 offset:10752\n"
    "s_bitcmp0_b32 s68, 0\n"
    "s_cselect_b32 s62, -1, 1\n"
    "v_mfma_f32_16x16x32_bf16 v[132:135], a[32:35], v[40:43], v[132:135]\n"
    "ds_read_b64_tr_b16 a[12:13], v7 offset:10240\n"
    "s_lshl_b32 s62, s62, 12\n"
    "v_mfma_f32_16x16x32_bf16 v[132:135], a[36:39], v[44:47], v[132:135]\n"
    "ds_read_b64_tr_b16 a[14:15], v7 offset:10752\n"
    "buffer_load_dwordx4 v[16:19], v195, s[8:11], s84 offen sc1\n"
    "v_mfma_f32_16x16x32_bf16 v[132:135], a[40:43], v[56:59], v[132:135]\n"
    "ds_read_b64_tr_b16 a[16:17], v4 offset:8192\n"
    "s_add_u32 s81, s64, s62\n"
    "v_mfma_f32_16x16x32_bf16 v[132:135], a[44:47], v[60:63], v[132:135]\n"
    "ds_read_b64_tr_b16 a[18:19], v4 offset:8704\n"
    "s_add_u32 s62, s65, s62\n"
    "v_mfma_f32_16x16x32_bf16 v[128:131], a[40:43], v[48:51], v[128:131]\n"
    "ds_read_b64_tr_b16 a[20:21], v8 offset:8192\n"
    "buffer_load_dwordx4 v[20:23], v195, s[8:11], s84 offen offset:1024 sc1\n"
    "v_mfma_f32_16x16x32_bf16 v[128:131], a[44:47], v[52:55], v[128:131]\n"
    "ds_read_b64_tr_b16 a[22:23], v8 offset:8704\n"
    "s_waitcnt lgkmcnt(4)\n"
    "v_mfma_f32_16x16x32_bf16 v[136:139], a[0:3], v[32:35], v[136:139]\n"
    "ds_read_b64_tr_b16 a[24:25], v4 offset:10240\n"
    "v_mul_f32 v175, v202, v175\n"
    "v_mfma_f32_16x16x32_bf16 v[136:139], a[4:7], v[36:39], v[136:139]\n"
    "ds_read_b64_tr_b16 a[26:27], v4 offset:10752\n"
    "buffer_load_dwordx4 v[24:27], v195, s[8:11], s84 offen offset:2048 sc1\n"
    "v_mfma_f32_16x16x32_bf16 v[140:143], a[0:3], v[40:43], v[140:143]\n"
    "ds_read_b64_tr_b16 a[28:29], v8 offset:10240\n"
    "v_mul_f32 v176, v200, v176\n"
    "v_mfma_f32_16x16x32_bf16 v[140:143], a[4:7], v[44:47], v[140:143]\n"
    "ds_read_b64_tr_b16 a[30:31], v8 offset:10752\n"
    "v_mul_f32 v177, v200, v177\n"
    "v_mfma_f32_16x16x32_bf16 v[140:143], a[8:11], v[56:59], v[140:143]\n"
    "ds_read_b64_tr_b16 a[32:33], v5 offset:8192\n"
    "buffer_load_dwordx4 v[28:31], v195, s[8:11], s84 offen offset:3072 sc1\n"
    "v_mfma_f32_16x16x32_bf16 v[140:143], a[12:15], v[60:63], v[140:143]\n"
    "ds_read_b64_tr_b16 a[34:35], v5 offset:8704\n"
    "s_xor_b32 s84, s99, 4096\n"
    "v_mfma_f32_16x16x32_bf16 v[136:139], a[8:11], v[48:51], v[136:139]\n"
    "ds_read_b64_tr_b16 a[36:37], v9 offset:8192\n"
    "v_mfma_f32_16x16x32_bf16 v[136:139], a[12:15], v[52:55], v[136:139]\n"
    "ds_read_b64_tr_b16 a[38:39], v9 offset:8704\n"
    "s_waitcnt lgkmcnt(4)\n"
    "s_barrier\n"
    "v_mfma_f32_16x16x32_bf16 v[144:147], a[16:19], v[32:35], v[144:147]\n"
    "ds_read_b64_tr_b16 a[40:41], v5 offset:10240\n"
    "v_mul_f32 v178, v200, v178\n"
    "v_mfma_f32_16x16x32_bf16 v[144:147], a[20:23], v[36:39], v[144:147]\n"
    "ds_read_b64_tr_b16 a[42:43], v5 offset:10752\n"
    "v_mul_f32 v179, v200, v179\n"
    "v_mfma_f32_16x16x32_bf16 v[148:151], a[16:19], v[40:43], v[148:151]\n"
    "ds_read_b64_tr_b16 a[44:45], v9 offset:10240\n"
    "v_mul_f32 v180, v202, v180\n"
    "v_mfma_f32_16x16x32_bf16 v[148:151], a[20:23], v[44:47], v[148:151]\n"
    "ds_read_b64_tr_b16 a[46:47], v9 offset:10752\n"
    "v_mul_f32 v181, v202, v181\n"
    "v_mfma_f32_16x16x32_bf16 v[148:151], a[24:27], v[56:59], v[148:151]\n"
    "ds_read_b64_tr_b16 a[0:1], v2 offset:12288\n"
    "v_mul_f32 v182, v202, v182\n"
    "v_mfma_f32_16x16x32_bf16 v[148:151], a[28:31], v[60:63], v[148:151]\n"
    "ds_read_b64_tr_b16 a[2:3], v2 offset:12800\n"
    "v_mul_f32 v183, v202, v183\n"
    "v_mfma_f32_16x16x32_bf16 v[144:147], a[24:27], v[48:51], v[144:147]\n"
    "ds_read_b64_tr_b16 a[4:5], v6 offset:12288\n"
    "v_mul_f32 v184, v200, v184\n"
    "v_mfma_f32_16x16x32_bf16 v[144:147], a[28:31], v[52:55], v[144:147]\n"
    "ds_read_b64_tr_b16 a[6:7], v6 offset:12800\n"
    "s_waitcnt lgkmcnt(4)\n"
    "v_mfma_f32_16x16x32_bf16 v[152:155], a[32:35], v[32:35], v[152:155]\n"
    "ds_read_b64_tr_b16 a[8:9], v2 offset:14336\n"
    "v_mul_f32 v185, v200, v185\n"
    "v_mfma_f32_16x16x32_bf16 v[152:155], a[36:39], v[36:39], v[152:155]\n"
    "ds_read_b64_tr_b16 a[10:11], v2 offset:14848\n"
    "v_mul_f32 v186, v200, v186\n"
    "v_mfma_f32_16x16x32_bf16 v[156:159], a[32:35], v[40:43], v[156:159]\n"
    "ds_read_b64_tr_b16 a[12:13], v6 offset:14336\n"
    "v_mul_f32 v187, v200, v187\n"
    "v_mfma_f32_16x16x32_bf16 v[156:159], a[36:39], v[44:47], v[156:159]\n"
    "ds_read_b64_tr_b16 a[14:15], v6 offset:14848\n"
    "v_mul_f32 v188, v202, v188\n"
    "v_mfma_f32_16x16x32_bf16 v[156:159], a[40:43], v[56:59], v[156:159]\n"
    "ds_read_b64_tr_b16 a[16:17], v3 offset:12288\n"
    "buffer_load_dwordx4 v[32:35], v195, s[8:11], s84 offen sc1\n"
    "v_mfma_f32_16x16x32_bf16 v[156:159], a[44:47], v[60:63], v[156:159]\n"
    "ds_read_b64_tr_b16 a[18:19], v3 offset:12800\n"
    "v_mul_f32 v189, v202, v189\n"
    "v_mfma_f32_16x16x32_bf16 v[152:155], a[40:43], v[48:51], v[152:155]\n"
    "ds_read_b64_tr_b16 a[20:21], v7 offset:12288\n"
    "v_mul_f32 v190, v202, v190\n"
    "v_mfma_f32_16x16x32_bf16 v[152:155], a[44:47], v[52:55], v[152:155]\n"
    "ds_read_b64_tr_b16 a[22:23], v7 offset:12800\n"
    "s_waitcnt lgkmcnt(4)\n"
    "v_mfma_f32_16x16x32_bf16 v[128:131], a[0:3], v[64:67], v[128:131]\n"
    "ds_read_b64_tr_b16 a[24:25], v3 offset:14336\n"
    "buffer_load_dwordx4 v[36:39], v195, s[8:11], s84 offen offset:1024 sc1\n"
    "v_mfma_f32_16x16x32_bf16 v[128:131], a[4:7], v[68:71], v[128:131]\n"
    "ds_read_b64_tr_b16 a[26:27], v3 offset:14848\n"
    "v_mul_f32 v191, v202, v191\n"
    "v_mfma_f32_16x16x32_bf16 v[132:135], a[0:3], v[72:75], v[132:135]\n"
    "ds_read_b64_tr_b16 a[28:29], v7 offset:14336\n"
    "s_waitcnt vmcnt(7)\n"
    "v_mfma_f32_16x16x32_bf16 v[132:135], a[4:7], v[76:79], v[132:135]\n"
    "ds_read_b64_tr_b16 a[30:31], v7 offset:14848\n"
    "buffer_load_dwordx4 v[40:43], v195, s[8:11], s84 offen offset:2048 sc1\n"
    "v_mfma_f32_16x16x32_bf16 v[132:135], a[8:11], v[88:91], v[132:135]\n"
    "ds_read_b64_tr_b16 a[32:33], v4 offset:12288\n"
    "v_max3_u32 v196, v196, v197, v198\n"
    "v_mfma_f32_16x16x32_bf16 v[132:135], a[12:15], v[92:95], v[132:135]\n"
    "ds_read_b64_tr_b16 a[34:35], v4 offset:12800\n"
    "v_max_u32 v196, v196, v199\n"
    "v_mfma_f32_16x16x32_bf16 v[128:131], a[8:11], v[80:83], v[128:131]\n"
    "ds_read_b64_tr_b16 a[36:37], v8 offset:12288\n"
    "buffer_load_dwordx4 v[44:47], v195, s[8:11], s84 offen offset:3072 sc1\n"
    "v_mfma_f32_16x16x32_bf16 v[128:131], a[12:15], v[84:87], v[128:131]\n"
    "ds_read_b64_tr_b16 a[38:39], v8 offset:12800\n"
    "s_waitcnt lgkmcnt(4)\n"
    "v_mfma_f32_16x16x32_bf16 v[136:139], a[16:19], v[64:67], v[136:139]\n"
    "ds_read_b64_tr_b16 a[40:41], v4 offset:14336\n"
    "s_waitcnt vmcnt(8)\n"
    "v_mfma_f32_16x16x32_bf16 v[136:139], a[20:23], v[68:71], v[136:139]\n"
    "ds_read_b64_tr_b16 a[42:43], v4 offset:14848\n"
    "v_cmp_ne_u32 vcc, 1, v220\n"
    "v_mfma_f32_16x16x32_bf16 v[140:143], a[16:19], v[72:75], v[140:143]\n"
    "ds_read_b64_tr_b16 a[44:45], v8 offset:14336\n"
    "s_cbranch_vccnz .Lo_19_%=\n"
    ".Lr_20_%=:\n"
    "v_mfma_f32_16x16x32_bf16 v[140:143], a[20:23], v[76:79], v[140:143]\n"
    "ds_read_b64_tr_b16 a[46:47], v8 offset:14848\n"
    "buffer_load_dwordx4 v[224:227], v222, s[8:11], s64 offen sc1\n"
    "v_mfma_f32_16x16x32_bf16 v[140:143], a[24:27], v[88:91], v[140:143]\n"
    "ds_read_b64_tr_b16 a[0:1], v5 offset:12288\n"
    "v_max_u32_dpp v196, v196, v196 row_ror:8 row_mask:0xf bank_mask:0xf\n"
    "v_mfma_f32_16x16x32_bf16 v[140:143], a[28:31], v[92:95], v[140:143]\n"
    "ds_read_b64_tr_b16 a[2:3], v5 offset:12800\n"
    "v_max_u32_dpp v196, v196, v196 row_ror:4 row_mask:0xf bank_mask:0xf\n"
    "v_mfma_f32_16x16x32_bf16 v[136:139], a[24:27], v[80:83], v[136:139]\n"
    "ds_read_b64_tr_b16 a[4:5], v9 offset:12288\n"
    "buffer_load_dwordx4 v[232:235], v222, s[8:11], s81 offen sc1\n"
    "v_mfma_f32_16x16x32_bf16 v[136:139], a[28:31], v[84:87], v[136:139]\n"
    "ds_read_b64_tr_b16 a[6:7], v9 offset:12800\n"
    "s_waitcnt lgkmcnt(4)\n"
    "v_mfma_f32_16x16x32_bf16 v[144:147], a[32:35], v[64:67], v[144:147]\n"
    "ds_read_b64_tr_b16 a[8:9], v5 offset:14336\n"
    "v_max_u32_dpp v196, v196, v196 row_ror:2 row_mask:0xf bank_mask:0xf\n"
    "v_mfma_f32_16x16x32_bf16 v[144:147], a[36:39], v[68:71], v[144:147]\n"
    "ds_read_b64_tr_b16 a[10:11], v5 offset:14848\n"
    "buffer_load_dwordx4 v[228:231], v222, s[8:11], s64 offen offset:2048 sc1\n"
    "v_mfma_f32_16x16x32_bf16 v[148:151], a[32:35], v[72:75], v[148:151]\n"
    "ds_read_b64_tr_b16 a[12:13], v9 offset:14336\n"
    "v_max_u32_dpp v196, v196, v196 row_ror:1 row_mask:0xf bank_mask:0xf\n"
    "v_mfma_f32_16x16x32_bf16 v[148:151], a[36:39], v[76:79], v[148:151]\n"
    "ds_read_b64_tr_b16 a[14:15], v9 offset:14848\n"
    "v_mov_b32 v197, v196\n"
    "v_mfma_f32_16x16x32_bf16 v[148:151], a[40:43], v[88:91], v[148:151]\n"
    "ds_read_b64_tr_b16 a[16:17], v2 offset:16384\n"
    "buffer_load_dwordx4 v[236:239], v222, s[8:11], s81 offen offset:2048 sc1\n"
    "v_mfma_f32_16x16x32_bf16 v[148:151], a[44:47], v[92:95], v[148:151]\n"
    "ds_read_b64_tr_b16 a[18:19], v2 offset:16896\n"
    "v_permlane16_swap_b32 v196, v197\n"
    "v_mfma_f32_16x16x32_bf16 v[144:147], a[40:43], v[80:83], v[144:147]\n"
    "ds_read_b64_tr_b16 a[20:21], v6 offset:16384\n"
    "v_max_u32 v196, v196, v197\n"
    "v_mfma_f32_16x16x32_bf16 v[144:147], a[44:47], v[84:87], v[144:147]\n"
    "ds_read_b64_tr_b16 a[22:23], v6 offset:16896\n"
    "s_waitcnt lgkmcnt(4)\n"
    "v_mfma_f32_16x16x32_bf16 v[152:155], a[0:3], v[64:67], v[152:155]\n"
    "ds_read_b64_tr_b16 a[24:25], v2 offset:18432\n"
    "buffer_load_dwordx4 v[240:243], v222, s[8:11], s65 offen sc1\n"
    "v_mfma_f32_16x16x32_bf16 v[152:155], a[4:7], v[68:71], v[152:155]\n"
    "ds_read_b64_tr_b16 a[26:27], v2 offset:18944\n"
    "v_mov_b32 v197, v196\n"
    "v_mfma_f32_16x16x32_bf16 v[156:159], a[0:3], v[72:75], v[156:159]\n"
    "ds_read_b64_tr_b16 a[28:29], v6 offset:18432\n"
    "v_permlane32_swap_b32 v196, v197\n"
    "v_mfma_f32_16x16x32_bf16 v[156:159], a[4:7], v[76:79], v[156:159]\n"
    "ds_read_b64_tr_b16 a[30:31], v6 offset:18944\n"
    "buffer_load_dwordx4 v[248:251], v222, s[8:11], s62 offen sc1\n"
    "v_mfma_f32_16x16x32_bf16 v[156:159], a[8:11], v[88:91], v[156:159]\n"
    "ds_read_b64_tr_b16 a[32:33], v3 offset:16384\n"
    "v_max_u32 v196, v196, v197\n"
    "v_mfma_f32_16x16x32_bf16 v[156:159], a[12:15], v[92:95], v[156:159]\n"
    "ds_read_b64_tr_b16 a[34:35], v3 offset:16896\n"
    "s_mul_i32 s59, s59, 14336\n"
    "v_mfma_f32_16x16x32_bf16 v[152:155], a[8:11], v[80:83], v[152:155]\n"
    "ds_read_b64_tr_b16 a[36:37], v7 offset:16384\n"
    "buffer_load_dwordx4 v[244:247], v222, s[8:11], s65 offen offset:2048 sc1\n"
    "v_mfma_f32_16x16x32_bf16 v[152:155], a[12:15], v[84:87], v[152:155]\n"
    "ds_read_b64_tr_b16 a[38:39], v7 offset:16896\n"
    "s_waitcnt lgkmcnt(4)\n"
    "v_mfma_f32_16x16x32_bf16 v[128:131], a[16:19], v[96:99], v[128:131]\n"
    "ds_read_b64_tr_b16 a[40:41], v3 offset:18432\n"
    "s_lshl_b32 s82, s88, 7\n"
    "v_mfma_f32_16x16x32_bf16 v[128:131], a[20:23], v[100:103], v[128:131]\n"
    "ds_read_b64_tr_b16 a[42:43], v3 offset:18944\n"
    "buffer_load_dwordx4 v[252:255], v222, s[8:11], s62 offen offset:2048 sc1\n"
    "v_mfma_f32_16x16x32_bf16 v[132:135], a[16:19], v[104:107], v[132:135]\n"
    "ds_read_b64_tr_b16 a[44:45], v7 offset:18432\n"
    "s_xor_b32 s84, s99, 8192\n"
    "v_mfma_f32_16x16x32_bf16 v[132:135], a[20:23], v[108:111], v[132:135]\n"
    "ds_read_b64_tr_b16 a[46:47], v7 offset:18944\n"
    "s_add_u32 s59, s59, s82\n"
    "v_mfma_f32_16x16x32_bf16 v[132:135], a[24:27], v[120:123], v[132:135]\n"
    "ds_read_b64_tr_b16 a[0:1], v4 offset:16384\n"
    "buffer_load_dwordx4 v[48:51], v195, s[8:11], s84 offen sc1\n"
    "v_mfma_f32_16x16x32_bf16 v[132:135], a[28:31], v[124:127], v[132:135]\n"
    "ds_read_b64_tr_b16 a[2:3], v4 offset:16896\n"
    "s_and_b32 s54, s54, 1\n"
    "v_mfma_f32_16x16x32_bf16 v[128:131], a[24:27], v[112:115], v[128:131]\n"
    "ds_read_b64_tr_b16 a[4:5], v8 offset:16384\n"
    "s_lshl3_add_u32 s54, s54, s69\n"
    "v_mfma_f32_16x16x32_bf16 v[128:131], a[28:31], v[116:119], v[128:131]\n"
    "ds_read_b64_tr_b16 a[6:7], v8 offset:16896\n"
    "s_waitcnt lgkmcnt(4)\n"
    "v_mfma_f32_16x16x32_bf16 v[136:139], a[32:35], v[96:99], v[136:139]\n"
    "ds_read_b64_tr_b16 a[8:9], v4 offset:18432\n"
    "buffer_load_dwordx4 v[52:55], v195, s[8:11], s84 offen offset:1024 sc1\n"
    "v_mfma_f32_16x16x32_bf16 v[136:139], a[36:39], v[100:103], v[136:139]\n"
    "ds_read_b64_tr_b16 a[10:11], v4 offset:18944\n"
    "s_lshl_b32 s54, s54, 7\n"
    "v_mfma_f32_16x16x32_bf16 v[140:143], a[32:35], v[104:107], v[140:143]\n"
    "ds_read_b64_tr_b16 a[12:13], v8 offset:18432\n"
    "s_add_u32 s54, s54, 491520\n"
    "v_mfma_f32_16x16x32_bf16 v[140:143], a[36:39], v[108:111], v[140:143]\n"
    "ds_read_b64_tr_b16 a[14:15], v8 offset:18944\n"
    "buffer_load_dwordx4 v[56:59], v195, s[8:11], s84 offen offset:2048 sc1\n"
    "v_mfma_f32_16x16x32_bf16 v[140:143], a[40:43], v[120:123], v[140:143]\n"
    "ds_read_b64_tr_b16 a[16:17], v5 offset:16384\n"
    "s_lshl_b32 s61, s61, 12\n"
    "v_mfma_f32_16x16x32_bf16 v[140:143], a[44:47], v[124:127], v[140:143]\n"
    "ds_read_b64_tr_b16 a[18:19], v5 offset:16896\n"
    "s_add_u32 s61, s61, 2621440\n"
    "v_mfma_f32_16x16x32_bf16 v[136:139], a[40:43], v[112:115], v[136:139]\n"
    "ds_read_b64_tr_b16 a[20:21], v9 offset:16384\n"
    "buffer_load_dwordx4 v[60:63], v195, s[8:11], s84 offen offset:3072 sc1\n"
    "v_mfma_f32_16x16x32_bf16 v[136:139], a[44:47], v[116:119], v[136:139]\n"
    "ds_read_b64_tr_b16 a[22:23], v9 offset:16896\n"
    "s_waitcnt lgkmcnt(4)\n"
    "v_mfma_f32_16x16x32_bf16 v[144:147], a[0:3], v[96:99], v[144:147]\n"
    "ds_read_b64_tr_b16 a[24:25], v5 offset:18432\n"
    "s_and_b32 s82, s68, 2\n"
    "v_mfma_f32_16x16x32_bf16 v[144:147], a[4:7], v[100:103], v[144:147]\n"
    "ds_read_b64_tr_b16 a[26:27], v5 offset:18944\n"
    "s_add_u32 s82, s82, s68\n"
    "v_mfma_f32_16x16x32_bf16 v[148:151], a[0:3], v[104:107], v[148:151]\n"
    "ds_read_b64_tr_b16 a[28:29], v9 offset:18432\n"
    "s_lshl3_add_u32 s82, s85, s82\n"
    "v_mfma_f32_16x16x32_bf16 v[148:151], a[4:7], v[108:111], v[148:151]\n"
    "ds_read_b64_tr_b16 a[30:31], v9 offset:18944\n"
    "s_lshl4_add_u32 s82, s82, s86\n"
    "v_mfma_f32_16x16x32_bf16 v[148:151], a[8:11], v[120:123], v[148:151]\n"
    "s_lshl_b32 s82, s82, 10\n"
    "v_mul_f32 v160, v128, v160\n"
    "v_mfma_f32_16x16x32_bf16 v[148:151], a[12:15], v[124:127], v[148:151]\n"
    "s_add_u32 s58, s82, 2621440\n"
    "v_mul_f32 v161, v129, v161\n"
    "v_mfma_f32_16x16x32_bf16 v[144:147], a[8:11], v[112:115], v[144:147]\n"
    "v_mul_f32 v162, v130, v162\n"
    "v_mul_f32 v163, v131, v163\n"
    "v_mfma_f32_16x16x32_bf16 v[144:147], a[12:15], v[116:119], v[144:147]\n"
    "v_mul_f32 v164, v132, v164\n"
    "s_waitcnt lgkmcnt(0)\n"
    "v_mfma_f32_16x16x32_bf16 v[152:155], a[16:19], v[96:99], v[152:155]\n"
    "v_mul_f32 v165, v133, v165\n"
    "v_mul_f32 v166, v134, v166\n"
    "v_mfma_f32_16x16x32_bf16 v[152:155], a[20:23], v[100:103], v[152:155]\n"
    "v_mul_f32 v167, v135, v167\n"
    "v_mul_f32 v168, v136, v168\n"
    "v_mfma_f32_16x16x32_bf16 v[156:159], a[16:19], v[104:107], v[156:159]\n"
    "v_mul_f32 v169, v137, v169\n"
    "v_mul_f32 v170, v138, v170\n"
    "v_mfma_f32_16x16x32_bf16 v[156:159], a[20:23], v[108:111], v[156:159]\n"
    "v_mul_f32 v171, v139, v171\n"
    "v_mul_f32 v172, v140, v172\n"
    "v_mfma_f32_16x16x32_bf16 v[156:159], a[24:27], v[120:123], v[156:159]\n"
    "v_mul_f32 v173, v141, v173\n"
    "v_mul_f32 v174, v142, v174\n"
    "v_mfma_f32_16x16x32_bf16 v[156:159], a[28:31], v[124:127], v[156:159]\n"
    "v_mul_f32 v175, v143, v175\n"
    "v_cvt_pk_bf16_f32 v128, v160, v161\n"
    "v_mfma_f32_16x16x32_bf16 v[152:155], a[24:27], v[112:115], v[152:155]\n"
    "v_cvt_pk_bf16_f32 v129, v162, v163\n"
    "v_cvt_pk_bf16_f32 v130, v168, v169\n"
    "v_mfma_f32_16x16x32_bf16 v[152:155], a[28:31], v[116:119], v[152:155]\n"
    "v_cvt_pk_bf16_f32 v131, v170, v171\n"
    "v_cvt_pk_bf16_f32 v132, v164, v165\n"
    "v_cvt_pk_bf16_f32 v133, v166, v167\n"
    "v_cvt_pk_bf16_f32 v134, v172, v173\n"
    "v_cvt_pk_bf16_f32 v135, v174, v175\n"
    "s_xor_b32 s80, s76, 65536\n"
    "buffer_store_dwordx4 v[128:131], v1, s[8:11], s76 offen sc1\n"
    "v_pk_mul_f32 v[176:177], v[144:145], v[176:177]\n"
    "v_pk_mul_f32 v[178:179], v[146:147], v[178:179]\n"
    "v_pk_mul_f32 v[184:185], v[152:153], v[184:185]\n"
    "v_pk_mul_f32 v[186:187], v[154:155], v[186:187]\n"
    "v_cvt_pk_bf16_f32 v144, v176, v177\n"
    "v_cvt_pk_bf16_f32 v145, v178, v179\n"
    "v_cvt_pk_bf16_f32 v146, v184, v185\n"
    "v_cvt_pk_bf16_f32 v147, v186, v187\n"
    "buffer_store_dwordx4 v[132:135], v1, s[8:11], s80 offen sc1\n"
    "v_pk_mul_f32 v[180:181], v[148:149], v[180:181]\n"
    "v_pk_mul_f32 v[182:183], v[150:151], v[182:183]\n"
    "v_pk_mul_f32 v[188:189], v[156:157], v[188:189]\n"
    "v_pk_mul_f32 v[190:191], v[158:159], v[190:191]\n"
    "v_cvt_pk_bf16_f32 v148, v180, v181\n"
    "v_cvt_pk_bf16_f32 v149, v182, v183\n"
    "v_cvt_pk_bf16_f32 v150, v188, v189\n"
    "v_cvt_pk_bf16_f32 v151, v190, v191\n"
    "; ---- attention set-up: per-row bound |q| |k|max as the softmax shift; the true row max whenever the bound is not provably safe\n"
    "s_waitcnt vmcnt(6)\n"
    "s_bitcmp0_b32 s68, 0\n"
    "s_cbranch_scc0 .Lvn_24_%=\n"
    "v_swap_b32 v224, v234\n"
    "v_swap_b32 v225, v235\n"
    "v_swap_b32 v228, v238\n"
    "v_swap_b32 v229, v239\n"
    "v_swap_b32 v240, v250\n"
    "v_swap_b32 v241, v251\n"
    "v_swap_b32 v244, v254\n"
    "v_swap_b32 v245, v255\n"
    "s_branch .Lve_23_%=\n"
    ".Lvn_24_%=:\n"
    "v_swap_b32 v226, v232\n"
    "v_swap_b32 v227, v233\n"
    "v_swap_b32 v230, v236\n"
    "v_swap_b32 v231, v237\n"
    "v_swap_b32 v242, v248\n"
    "v_swap_b32 v243, v249\n"
    "v_swap_b32 v246, v252\n"
    "v_swap_b32 v247, v253\n"
    ".Lvn_25_%=:\n"
    ".Lve_23_%=:\n"
    "buffer_store_dwordx4 v[144:147], v1, s[8:11], s76 offen offset:1024 sc1\n"
    "v_dot2_f32_bf16 v2, v224, v224, 0\n"
    "v_dot2_f32_bf16 v4, v232, v232, 0\n"
    "v_dot2_f32_bf16 v3, v240, v240, 0\n"
    "v_dot2_f32_bf16 v5, v248, v248, 0\n"
    "v_dot2_f32_bf16 v2, v225, v225, v2\n"
    "v_dot2_f32_bf16 v4, v233, v233, v4\n"
    "v_dot2_f32_bf16 v3, v241, v241, v3\n"
    "v_dot2_f32_bf16 v5, v249, v249, v5\n"
    "v_dot2_f32_bf16 v2, v226, v226, v2\n"
    "v_dot2_f32_bf16 v4, v234, v234, v4\n"
    "v_dot2_f32_bf16 v3, v242, v242, v3\n"
    "v_dot2_f32_bf16 v5, v250, v250, v5\n"
    "buffer_store_dwordx4 v[148:151], v1, s[8:11], s80 offen offset:1024 sc1\n"
    "v_dot2_f32_bf16 v2, v227, v227, v2\n"
    "v_dot2_f32_bf16 v4, v235, v235, v4\n"
    "v_dot2_f32_bf16 v3, v243, v243, v3\n"
    "v_dot2_f32_bf16 v5, v251, v251, v5\n"
    "v_dot2_f32_bf16 v2, v228, v228, v2\n"
    "v_dot2_f32_bf16 v4, v236, v236, v4\n"
    "v_dot2_f32_bf16 v3, v244, v244, v3\n"
    "v_dot2_f32_bf16 v5, v252, v252, v5\n"
    "v_dot2_f32_bf16 v2, v229, v229, v2\n"
    "v_dot2_f32_bf16 v4, v237, v237, v4\n"
    "v_dot2_f32_bf16 v3, v245, v245, v3\n"
    "v_dot2_f32_bf16 v5, v253, v253, v5\n"
    "s_mov_b32 exec_hi, 0\n"
    "v_dot2_f32_bf16 v2, v230, v230, v2\n"
    "v_dot2_f32_bf16 v4, v238, v238, v4\n"
    "v_dot2_f32_bf16 v3, v246, v246, v3\n"
    "v_dot2_f32_bf16 v5, v254, v254, v5\n"
    "v_dot2_f32_bf16 v2, v231, v231, v2\n"
    "v_dot2_f32_bf16 v4, v239, v239, v4\n"
    "v_dot2_f32_bf16 v3, v247, v247, v3\n"
    "v_dot2_f32_bf16 v5, v255, v255, v5\n"
    "s_not_b64 exec, exec\n"
    "v_mov_b32 v231, 0\n"
    "v_mov_b32 v239, 0\n"
    "v_mov_b32 v247, 0\n"
    "v_mov_b32 v255, 0\n"
    "s_mov_b32 exec_lo, -1\n"
    "v_permlane32_swap_b32 v2, v4\n"
    "v_permlane32_swap_b32 v3, v5\n"
    "v_pk_add_f32 v[2:3], v[2:3], v[4:5]\n"
    "v_pk_mul_f32 v[2:3], v[2:3], v[196:197] op_sel_hi:[1,0]\n"
    "; guard: bound^2 <= 1600 in every row, else the slow path\n"
    "v_max_u32 v6, v2, v3\n"
    "v_cmp_lt_u32 vcc, 0x44c80000, v6\n"
    "v_sqrt_f32 v2, v2\n"
    "v_sqrt_f32 v3, v3\n"
    "s_cbranch_vccnz .Lfb_26_%=\n"
    "v_cvt_pk_bf16_f32 v2, -v2, 0\n"
    "v_cvt_pk_bf16_f32 v3, -v3, 0\n"
    "s_mov_b32 exec_lo, 0\n"
    "v_mov_b32 v238, v2\n"
    "v_mov_b32 v254, v3\n"
    "s_mov_b32 exec_lo, -1\n"
    "v_permlane32_swap_b32 v230, v2\n"
    "v_permlane32_swap_b32 v246, v3\n"
    ".Lfr_27_%=:\n"
    "; ---- attention: 16 key tiles x 4 query tiles per wave\n"
    "v_mfma_f32_32x32x16_bf16 v[80:95], v[16:19], v[224:227], 0\n"
    "v_mfma_f32_32x32x16_bf16 v[80:95], v[20:23], v[228:231], v[80:95]\n"
    "v_and_b32 v218, 7, v0\n"
    "v_lshlrev_b32 v218, 12, v218\n"
    "v_and_b32 v221, 8, v0\n"
    "v_lshl_add_u32 v218, v221, 6, v218\n"
    "v_and_b32 v221, 240, v1\n"
    "v_lshlrev_b32 v221, 7, v221\n"
    "v_lshrrev_b32 v199, 3, v0\n"
    "v_mul_u32_u24 v199, 896, v199\n"
    "v_and_b32 v192, 112, v1\n"
    "v_add_u32 v199, v199, v192\n"
    "s_add_u32 s62, s59, 7168\n"
    "v_mfma_f32_32x32x16_bf16 v[96:111], v[16:19], v[232:235], 0\n"
    "v_exp_f32 v120, v80\n"
    "v_exp_f32 v121, v81\n"
    "v_exp_f32 v122, v82\n"
    "v_exp_f32 v123, v83\n"
    "v_cvt_pk_bf16_f32 v112, v120, v121\n"
    "s_xor_b32 s84, s99, 12288\n"
    "v_exp_f32 v124, v84\n"
    "v_exp_f32 v125, v85\n"
    "v_cvt_pk_bf16_f32 v113, v122, v123\n"
    "v_exp_f32 v120, v86\n"
    "v_exp_f32 v121, v87\n"
    "v_cvt_pk_bf16_f32 v114, v124, v125\n"
    "v_mfma_f32_32x32x16_bf16 v[96:111], v[20:23], v[236:239], v[96:111]\n"
    "buffer_load_dwordx4 v[64:67], v195, s[8:11], s84 offen sc1\n"
    "v_exp_f32 v122, v88\n"
    "v_exp_f32 v123, v89\n"
    "v_cvt_pk_bf16_f32 v115, v120, v121\n"
    "v_exp_f32 v124, v90\n"
    "v_exp_f32 v125, v91\n"
    "v_cvt_pk_bf16_f32 v116, v122, v123\n"
    "v_mfma_f32_32x32x16_bf16 v[128:143], v[24:27], v[112:115], 0\n"
    "v_exp_f32 v120, v92\n"
    "v_exp_f32 v121, v93\n"
    "v_cvt_pk_bf16_f32 v117, v124, v125\n"
    "v_exp_f32 v122, v94\n"
    "v_exp_f32 v123, v95\n"
    "v_cvt_pk_bf16_f32 v118, v120, v121\n"
    "v_mfma_f32_32x32x16_bf16 v[80:95], v[16:19], v[240:243], 0\n"
    "v_exp_f32 v124, v96\n"
    "v_exp_f32 v125, v97\n"
    "v_cvt_pk_bf16_f32 v119, v122, v123\n"
    "v_exp_f32 v120, v98\n"
    "v_exp_f32 v121, v99\n"
    "v_cvt_pk_bf16_f32 v112, v124, v125\n"
    "v_mfma_f32_32x32x16_bf16 v[128:143], v[28:31], v[116:119], v[128:143]\n"
    "v_exp_f32 v122, v100\n"
    "v_exp_f32 v123, v101\n"
    "v_cvt_pk_bf16_f32 v113, v120, v121\n"
    "v_exp_f32 v124, v102\n"
    "v_exp_f32 v125, v103\n"
    "v_cvt_pk_bf16_f32 v114, v122, v123\n"
    "v_mfma_f32_32x32x16_bf16 v[80:95], v[20:23], v[244:247], v[80:95]\n"
    "buffer_load_dwordx4 v[68:71], v195, s[8:11], s84 offen offset:1024 sc1\n"
    "v_exp_f32 v120, v104\n"
    "v_exp_f32 v121, v105\n"
    "v_cvt_pk_bf16_f32 v115, v124, v125\n"
    "v_exp_f32 v122, v106\n"
    "v_exp_f32 v123, v107\n"
    "v_cvt_pk_bf16_f32 v116, v120, v121\n"
    "v_mfma_f32_32x32x16_bf16 v[144:159], v[24:27], v[112:115], 0\n"
    "v_exp_f32 v124, v108\n"
    "v_exp_f32 v125, v109\n"
    "v_cvt_pk_bf16_f32 v117, v122, v123\n"
    "v_exp_f32 v120, v110\n"
    "v_exp_f32 v121, v111\n"
    "v_cvt_pk_bf16_f32 v118, v124, v125\n"
    "v_mfma_f32_32x32x16_bf16 v[96:111], v[16:19], v[248:251], 0\n"
    "v_exp_f32 v122, v80\n"
    "v_exp_f32 v123, v81\n"
    "v_cvt_pk_bf16_f32 v119, v120, v121\n"
    "v_exp_f32 v124, v82\n"
    "v_exp_f32 v125, v83\n"
    "v_cvt_pk_bf16_f32 v112, v122, v123\n"
    "v_mfma_f32_32x32x16_bf16 v[144:159], v[28:31], v[116:119], v[144:159]\n"
    "v_exp_f32 v120, v84\n"
    "v_exp_f32 v121, v85\n"
    "v_cvt_pk_bf16_f32 v113, v124, v125\n"
    "v_exp_f32 v122, v86\n"
    "v_exp_f32 v123, v87\n"
    "v_cvt_pk_bf16_f32 v114, v120, v121\n"
    "v_mfma_f32_32x32x16_bf16 v[96:111], v[20:23], v[252:255], v[96:111]\n"
    "buffer_load_dwordx4 v[72:75], v195, s[8:11], s84 offen offset:2048 sc1\n"
    "v_exp_f32 v124, v88\n"
    "v_exp_f32 v125, v89\n"
    "v_cvt_pk_bf16_f32 v115, v122, v123\n"
    "v_exp_f32 v120, v90\n"
    "v_exp_f32 v121, v91\n"
    "v_cvt_pk_bf16_f32 v116, v124, v125\n"
    "v_mfma_f32_32x32x16_bf16 v[160:175], v[24:27], v[112:115], 0\n"
    "v_exp_f32 v122, v92\n"
    "v_exp_f32 v123, v93\n"
    "v_cvt_pk_bf16_f32 v117, v120, v121\n"
    "v_exp_f32 v124, v94\n"
    "v_exp_f32 v125, v95\n"
    "v_cvt_pk_bf16_f32 v118, v122, v123\n"
    "v_mfma_f32_32x32x16_bf16 v[80:95], v[32:35], v[224:227], 0\n"
    "s_add_u32 m0, s78, 20480\n"
    "v_exp_f32 v120, v96\n"
    "v_exp_f32 v121, v97\n"
    "v_cvt_pk_bf16_f32 v119, v124, v125\n"
    "v_exp_f32 v122, v98\n"
    "v_exp_f32 v123, v99\n"
    "v_cvt_pk_bf16_f32 v112, v120, v121\n"
    "v_mfma_f32_32x32x16_bf16 v[160:175], v[28:31], v[116:119], v[160:175]\n"
    "buffer_load_dwordx4 v204, s[20:23], s36 offen lds\n"
    "v_exp_f32 v124, v100\n"
    ".Lpage_4_%=:\n"
    "s_cbranch_execz .Lpage_8_%=\n"
    "v_exp_f32 v125, v101\n"
    "v_cvt_pk_bf16_f32 v113, v122, v123\n"
    "v_exp_f32 v120, v102\n"
    "v_exp_f32 v121, v103\n"
    "v_cvt_pk_bf16_f32 v114, v124, v125\n"
    "v_mfma_f32_32x32x16_bf16 v[80:95], v[36:39], v[228:231], v[80:95]\n"
    "buffer_load_dwordx4 v[76:79], v195, s[8:11], s84 offen offset:3072 sc1\n"
    "v_exp_f32 v122, v104\n"
    "v_exp_f32 v123, v105\n"
    "v_cvt_pk_bf16_f32 v115, v120, v121\n"
    "v_exp_f32 v124, v106\n"
    "v_exp_f32 v125, v107\n"
    "v_cvt_pk_bf16_f32 v116, v122, v123\n"
    "v_mfma_f32_32x32x16_bf16 v[176:191], v[24:27], v[112:115], 0\n"
    "buffer_load_dwordx4 v205, s[20:23], s36 offen offset:1024 lds\n"
    "v_exp_f32 v120, v108\n"
    "v_exp_f32 v121, v109\n"
    "v_cvt_pk_bf16_f32 v117, v124, v125\n"
    "v_exp_f32 v122, v110\n"
    "v_exp_f32 v123, v111\n"
    "v_cvt_pk_bf16_f32 v118, v120, v121\n"
    "v_mfma_f32_32x32x16_bf16 v[96:111], v[32:35], v[232:235], 0\n"
    "v_exp_f32 v124, v80\n"
    "v_exp_f32 v125, v81\n"
    "v_cvt_pk_bf16_f32 v119, v122, v123\n"
    "v_exp_f32 v120, v82\n"
    "v_exp_f32 v121, v83\n"
    "v_cvt_pk_bf16_f32 v112, v124, v125\n"
    "v_mfma_f32_32x32x16_bf16 v[176:191], v[28:31], v[116:119], v[176:191]\n"
    "s_xor_b32 s84, s99, 16384\n"
    "v_exp_f32 v122, v84\n"
    "v_exp_f32 v123, v85\n"
    "v_cvt_pk_bf16_f32 v113, v120, v121\n"
    "v_exp_f32 v124, v86\n"
    "v_exp_f32 v125, v87\n"
    "v_cvt_pk_bf16_f32 v114, v122, v123\n"
    "v_mfma_f32_32x32x16_bf16 v[96:111], v[36:39], v[236:239], v[96:111]\n"
    "buffer_load_dwordx4 v[16:19], v195, s[8:11], s84 offen sc1\n"
    "v_exp_f32 v120, v88\n"
    "v_exp_f32 v121, v89\n"
    "v_cvt_pk_bf16_f32 v115, v124, v125\n"
    "v_exp_f32 v122, v90\n"
    "v_exp_f32 v123, v91\n"
    "v_cvt_pk_bf16_f32 v116, v120, v121\n"
    "v_mfma_f32_32x32x16_bf16 v[128:143], v[40:43], v[112:115], v[128:143]\n"
    "buffer_load_dwordx4 v206, s[20:23], s36 offen offset:2048 lds\n"
    "v_exp_f32 v124, v92\n"
    "v_exp_f32 v125, v93\n"
    "v_cvt_pk_bf16_f32 v117, v122, v123\n"
    "v_exp_f32 v120, v94\n"
    "v_exp_f32 v121, v95\n"
    "v_cvt_pk_bf16_f32 v118, v124, v125\n"
    "v_mfma_f32_32x32x16_bf16 v[80:95], v[32:35], v[240:243], 0\n"
    "buffer_load_dwordx4 v207, s[20:23], s36 offen offset:3072 lds\n"
    "v_exp_f32 v122, v96\n"
    "v_exp_f32 v123, v97\n"
    "v_cvt_pk_bf16_f32 v119, v120, v121\n"
    "v_exp_f32 v124, v98\n"
    "v_exp_f32 v125, v99\n"
    "v_cvt_pk_bf16_f32 v112, v122, v123\n"
    "v_mfma_f32_32x32x16_bf16 v[128:143], v[44:47], v[116:119], v[128:143]\n"
    "v_exp_f32 v120, v100\n"
    "v_exp_f32 v121, v101\n"
    "v_cvt_pk_bf16_f32 v113, v124, v125\n"
    "v_exp_f32 v122, v102\n"
    "v_exp_f32 v123, v103\n"
    "v_cvt_pk_bf16_f32 v114, v120, v121\n"
    "v_mfma_f32_32x32x16_bf16 v[80:95], v[36:39], v[244:247], v[80:95]\n"
    "buffer_load_dwordx4 v[20:23], v195, s[8:11], s84 offen offset:1024 sc1\n"
    "v_exp_f32 v124, v104\n"
    "v_exp_f32 v125, v105\n"
    "v_cvt_pk_bf16_f32 v115, v122, v123\n"
    "v_exp_f32 v120, v106\n"
    "v_exp_f32 v121, v107\n"
    "v_cvt_pk_bf16_f32 v116, v124, v125\n"
    "v_mfma_f32_32x32x16_bf16 v[144:159], v[40:43], v[112:115], v[144:159]\n"
    "v_exp_f32 v122, v108\n"
    "v_exp_f32 v123, v109\n"
    "v_cvt_pk_bf16_f32 v117, v120, v121\n"
    "v_exp_f32 v124, v110\n"
    "v_exp_f32 v125, v111\n"
    "v_cvt_pk_bf16_f32 v118, v122, v123\n"
    "v_mfma_f32_32x32x16_bf16 v[96:111], v[32:35], v[248:251], 0\n"
    "s_add_u32 s36, s36, 28672\n"
    "v_exp_f32 v120, v80\n"
    "v_exp_f32 v121, v81\n"
    "v_cvt_pk_bf16_f32 v119, v124, v125\n"
    "v_exp_f32 v122, v82\n"
    "v_exp_f32 v123, v83\n"
    "v_cvt_pk_bf16_f32 v112, v120, v121\n"
    "v_mfma_f32_32x32x16_bf16 v[144:159], v[44:47], v[116:119], v[144:159]\n"
    "s_add_u32 m0, s78, 24576\n"
    "v_exp_f32 v124, v84\n"
    "v_exp_f32 v125, v85\n"
    "v_cvt_pk_bf16_f32 v113, v122, v123\n"
    "v_exp_f32 v120, v86\n"
    "v_exp_f32 v121, v87\n"
    "v_cvt_pk_bf16_f32 v114, v124, v125\n"
    "v_mfma_f32_32x32x16_bf16 v[96:111], v[36:39], v[252:255], v[96:111]\n"
    "buffer_load_dwordx4 v[24:27], v195, s[8:11], s84 offen offset:2048 sc1\n"
    "v_exp_f32 v122, v88\n"
    "v_exp_f32 v123, v89\n"
    "v_cvt_pk_bf16_f32 v115, v120, v121\n"
    "v_exp_f32 v124, v90\n"
    "v_exp_f32 v125, v91\n"
    "v_cvt_pk_bf16_f32 v116, v122, v123\n"
    "v_mfma_f32_32x32x16_bf16 v[160:175], v[40:43], v[112:115], v[160:175]\n"
    "s_waitcnt vmcnt(15)\n"
    "v_exp_f32 v120, v92\n"
    "v_exp_f32 v121, v93\n"
    "v_cvt_pk_bf16_f32 v117, v124, v125\n"
    "v_exp_f32 v122, v94\n"
    "v_exp_f32 v123, v95\n"
    "v_cvt_pk_bf16_f32 v118, v120, v121\n"
    "v_mfma_f32_32x32x16_bf16 v[80:95], v[48:51], v[224:227], 0\n"
    "buffer_load_dwordx4 v204, s[20:23], s36 offen lds\n"
    "v_exp_f32 v124, v96\n"
    "v_exp_f32 v125, v97\n"
    "v_cvt_pk_bf16_f32 v119, v122, v123\n"
    "v_exp_f32 v120, v98\n"
    "v_exp_f32 v121, v99\n"
    "v_cvt_pk_bf16_f32 v112, v124, v125\n"
    "v_mfma_f32_32x32x16_bf16 v[160:175], v[44:47], v[116:119], v[160:175]\n"
    "buffer_load_dwordx4 v205, s[20:23], s36 offen offset:1024 lds\n"
    "v_exp_f32 v122, v100\n"
    "v_exp_f32 v123, v101\n"
    "v_cvt_pk_bf16_f32 v113, v120, v121\n"
    "v_exp_f32 v124, v102\n"
    "v_exp_f32 v125, v103\n"
    "v_cvt_pk_bf16_f32 v114, v122, v123\n"
    "v_mfma_f32_32x32x16_bf16 v[80:95], v[52:55], v[228:231], v[80:95]\n"
    "buffer_load_dwordx4 v[28:31], v195, s[8:11], s84 offen offset:3072 sc1\n"
    "v_exp_f32 v120, v104\n"
    "v_exp_f32 v121, v105\n"
    "v_cvt_pk_bf16_f32 v115, v124, v125\n"
    "v_exp_f32 v122, v106\n"
    "v_exp_f32 v123, v107\n"
    "v_cvt_pk_bf16_f32 v116, v120, v121\n"
    "v_mfma_f32_32x32x16_bf16 v[176:191], v[40:43], v[112:115], v[176:191]\n"
    "v_exp_f32 v124, v108\n"
    "v_exp_f32 v125, v109\n"
    "v_cvt_pk_bf16_f32 v117, v122, v123\n"
    "v_exp_f32 v120, v110\n"
    "v_exp_f32 v121, v111\n"
    "v_cvt_pk_bf16_f32 v118, v124, v125\n"
    "v_mfma_f32_32x32x16_bf16 v[96:111], v[48:51], v[232:235], 0\n"
    "buffer_load_dwordx4 v206, s[20:23], s36 offen offset:2048 lds\n"
    "v_exp_f32 v122, v80\n"
    "v_exp_f32 v123, v81\n"
    "v_cvt_pk_bf16_f32 v119, v120, v121\n"
    "v_exp_f32 v124, v82\n"
    "v_exp_f32 v125, v83\n"
    "v_cvt_pk_bf16_f32 v112, v122, v123\n"
    "v_mfma_f32_32x32x16_bf16 v[176:191], v[44:47], v[116:119], v[176:191]\n"
    "s_xor_b32 s84, s99, 20480\n"
    "v_exp_f32 v120, v84\n"
    "v_exp_f32 v121, v85\n"
    "v_cvt_pk_bf16_f32 v113, v124, v125\n"
    "v_exp_f32 v122, v86\n"
    "v_exp_f32 v123, v87\n"
    "v_cvt_pk_bf16_f32 v114, v120, v121\n"
    "v_mfma_f32_32x32x16_bf16 v[96:111], v[52:55], v[236:239], v[96:111]\n"
    "buffer_load_dwordx4 v[32:35], v195, s[8:11], s84 offen sc1\n"
    "v_exp_f32 v124, v88\n"
    "v_exp_f32 v125, v89\n"
    "v_cvt_pk_bf16_f32 v115, v122, v123\n"
    "v_exp_f32 v120, v90\n"
    "v_exp_f32 v121, v91\n"
    "v_cvt_pk_bf16_f32 v116, v124, v125\n"
    "v_mfma_f32_32x32x16_bf16 v[128:143], v[56:59], v[112:115], v[128:143]\n"
    "buffer_load_dwordx4 v207, s[20:23], s36 offen offset:3072 lds\n"
    "v_exp_f32 v122, v92\n"
    "v_exp_f32 v123, v93\n"
    "v_cvt_pk_bf16_f32 v117, v120, v121\n"
    "v_exp_f32 v124, v94\n"
    "v_exp_f32 v125, v95\n"
    "v_cvt_pk_bf16_f32 v118, v122, v123\n"
    "v_mfma_f32_32x32x16_bf16 v[80:95], v[48:51], v[240:243], 0\n"
    "s_add_u32 m0, s78, 36864\n"
    "v_exp_f32 v120, v96\n"
    "v_exp_f32 v121, v97\n"
    "v_cvt_pk_bf16_f32 v119, v124, v125\n"
    "v_exp_f32 v122, v98\n"
    "v_exp_f32 v123, v99\n"
    "v_cvt_pk_bf16_f32 v112, v120, v121\n"
    "v_mfma_f32_32x32x16_bf16 v[128:143], v[60:63], v[116:119], v[128:143]\n"
    "buffer_load_dwordx4 v204, s[20:23], s37 offen lds\n"
    "v_exp_f32 v124, v100\n"
    "v_exp_f32 v125, v101\n"
    "v_cvt_pk_bf16_f32 v113, v122, v123\n"
    "v_exp_f32 v120, v102\n"
    "v_exp_f32 v121, v103\n"
    "v_cvt_pk_bf16_f32 v114, v124, v125\n"
    "v_mfma_f32_32x32x16_bf16 v[80:95], v[52:55], v[244:247], v[80:95]\n"
    "buffer_load_dwordx4 v[36:39], v195, s[8:11], s84 offen offset:1024 sc1\n"
    "v_exp_f32 v122, v104\n"
    "v_exp_f32 v123, v105\n"
    "v_cvt_pk_bf16_f32 v115, v120, v121\n"
    "v_exp_f32 v124, v106\n"
    "v_exp_f32 v125, v107\n"
    "v_cvt_pk_bf16_f32 v116, v122, v123\n"
    "v_mfma_f32_32x32x16_bf16 v[144:159], v[56:59], v[112:115], v[144:159]\n"
    "buffer_load_dwordx4 v205, s[20:23], s37 offen offset:1024 lds\n"
    "v_exp_f32 v120, v108\n"
    "v_exp_f32 v121, v109\n"
    "v_cvt_pk_bf16_f32 v117, v124, v125\n"
    "v_exp_f32 v122, v110\n"
    "v_exp_f32 v123, v111\n"
    "v_cvt_pk_bf16_f32 v118, v120, v121\n"
    "v_mfma_f32_32x32x16_bf16 v[96:111], v[48:51], v[248:251], 0\n"
    "s_waitcnt vmcnt(20)\n"
    "v_exp_f32 v124, v80\n"
    "v_exp_f32 v125, v81\n"
    "v_cvt_pk_bf16_f32 v119, v122, v123\n"
    "v_exp_f32 v120, v82\n"
    "v_exp_f32 v121, v83\n"
    "v_cvt_pk_bf16_f32 v112, v124, v125\n"
    "v_mfma_f32_32x32x16_bf16 v[144:159], v[60:63], v[116:119], v[144:159]\n"
    "s_barrier\n"
    "v_exp_f32 v122, v84\n"
    "v_exp_f32 v123, v85\n"
    "v_cvt_pk_bf16_f32 v113, v120, v121\n"
    "v_exp_f32 v124, v86\n"
    "v_exp_f32 v125, v87\n"
    "v_cvt_pk_bf16_f32 v114, v122, v123\n"
    "v_mfma_f32_32x32x16_bf16 v[96:111], v[52:55], v[252:255], v[96:111]\n"
    "buffer_load_dwordx4 v[40:43], v195, s[8:11], s84 offen offset:2048 sc1\n"
    "v_exp_f32 v120, v88\n"
    "v_exp_f32 v121, v89\n"
    "v_cvt_pk_bf16_f32 v115, v124, v125\n"
    "v_exp_f32 v122, v90\n"
    "v_exp_f32 v123, v91\n"
    "v_cvt_pk_bf16_f32 v116, v120, v121\n"
    "v_mfma_f32_32x32x16_bf16 v[160:175], v[56:59], v[112:115], v[160:175]\n"
    "s_waitcnt vmcnt(16)\n"
    "v_exp_f32 v124, v92\n"
    "v_exp_f32 v125, v93\n"
    "v_cvt_pk_bf16_f32 v117, v122, v123\n"
    "v_exp_f32 v120, v94\n"
    "v_exp_f32 v121, v95\n"
    "v_cvt_pk_bf16_f32 v118, v124, v125\n"
    "v_mfma_f32_32x32x16_bf16 v[80:95], v[64:67], v[224:227], 0\n"
    "buffer_store_dword v219, v216, s[8:11], s66 offen sc1\n"
    "v_exp_f32 v122, v96\n"
    "v_exp_f32 v123, v97\n"
    "v_cvt_pk_bf16_f32 v119, v120, v121\n"
    "v_exp_f32 v124, v98\n"
    "v_exp_f32 v125, v99\n"
    "v_cvt_pk_bf16_f32 v112, v122, v123\n"
    "v_mfma_f32_32x32x16_bf16 v[160:175], v[60:63], v[116:119], v[160:175]\n"
    "buffer_load_dwordx4 v206, s[20:23], s37 offen offset:2048 lds\n"
    "v_exp_f32 v120, v100\n"
    "v_exp_f32 v121, v101\n"
    "v_cvt_pk_bf16_f32 v113, v124, v125\n"
    "v_exp_f32 v122, v102\n"
    "v_exp_f32 v123, v103\n"
    "v_cvt_pk_bf16_f32 v114, v120, v121\n"
    "v_mfma_f32_32x32x16_bf16 v[80:95], v[68:71], v[228:231], v[80:95]\n"
    "buffer_load_dwordx4 v[44:47], v195, s[8:11], s84 offen offset:3072 sc1\n"
    "v_exp_f32 v124, v104\n"
    "v_exp_f32 v125, v105\n"
    "v_cvt_pk_bf16_f32 v115, v122, v123\n"
    "v_exp_f32 v120, v106\n"
    "v_exp_f32 v121, v107\n"
    "v_cvt_pk_bf16_f32 v116, v124, v125\n"
    "v_mfma_f32_32x32x16_bf16 v[176:191], v[56:59], v[112:115], v[176:191]\n"
    "v_exp_f32 v122, v108\n"
    "v_exp_f32 v123, v109\n"
    "v_cvt_pk_bf16_f32 v117, v120, v121\n"
    "v_exp_f32 v124, v110\n"
    "v_exp_f32 v125, v111\n"
    "v_cvt_pk_bf16_f32 v118, v122, v123\n"
    "v_mfma_f32_32x32x16_bf16 v[96:111], v[64:67], v[232:235], 0\n"
    "buffer_load_dwordx4 v207, s[20:23], s37 offen offset:3072 lds\n"
    "v_exp_f32 v120, v80\n"
    "v_exp_f32 v121, v81\n"
    "v_cvt_pk_bf16_f32 v119, v124, v125\n"
    "v_exp_f32 v122, v82\n"
    "v_exp_f32 v123, v83\n"
    "v_cvt_pk_bf16_f32 v112, v120, v121\n"
    "v_mfma_f32_32x32x16_bf16 v[176:191], v[60:63], v[116:119], v[176:191]\n"
    "s_xor_b32 s84, s99, 24576\n"
    "v_exp_f32 v124, v84\n"
    "v_exp_f32 v125, v85\n"
    "v_cvt_pk_bf16_f32 v113, v122, v123\n"
    "v_exp_f32 v120, v86\n"
    "v_exp_f32 v121, v87\n"
    "v_cvt_pk_bf16_f32 v114, v124, v125\n"
    "v_mfma_f32_32x32x16_bf16 v[96:111], v[68:71], v[236:239], v[96:111]\n"
    "buffer_load_dwordx4 v[48:51], v195, s[8:11], s84 offen sc1\n"
    "v_exp_f32 v122, v88\n"
    "v_exp_f32 v123, v89\n"
    "v_cvt_pk_bf16_f32 v115, v120, v121\n"
    "v_exp_f32 v124, v90\n"
    "v_exp_f32 v125, v91\n"
    "v_cvt_pk_bf16_f32 v116, v122, v123\n"
    "v_mfma_f32_32x32x16_bf16 v[128:143], v[72:75], v[112:115], v[128:143]\n"
    "s_add_u32 s37, s37, 28672\n"
    "v_exp_f32 v120, v92\n"
    "v_exp_f32 v121, v93\n"
    "v_cvt_pk_bf16_f32 v117, v124, v125\n"
    "v_exp_f32 v122, v94\n"
    "v_exp_f32 v123, v95\n"
    "v_cvt_pk_bf16_f32 v118, v120, v121\n"
    "v_mfma_f32_32x32x16_bf16 v[80:95], v[64:67], v[240:243], 0\n"
    "s_add_u32 m0, s78, 28672\n"
    "v_exp_f32 v124, v96\n"
    "v_exp_f32 v125, v97\n"
    "v_cvt_pk_bf16_f32 v119, v122, v123\n"
    "v_exp_f32 v120, v98\n"
    "v_exp_f32 v121, v99\n"
    "v_cvt_pk_bf16_f32 v112, v124, v125\n"
    "v_mfma_f32_32x32x16_bf16 v[128:143], v[76:79], v[116:119], v[128:143]\n"
    "buffer_load_dwordx4 v204, s[20:23], s37 offen lds\n"
    "v_exp_f32 v122, v100\n"
    "v_exp_f32 v123, v101\n"
    "v_cvt_pk_bf16_f32 v113, v120, v121\n"
    "v_exp_f32 v124, v102\n"
    "v_exp_f32 v125, v103\n"
    "v_cvt_pk_bf16_f32 v114, v122, v123\n"
    "v_mfma_f32_32x32x16_bf16 v[80:95], v[68:71], v[244:247], v[80:95]\n"
    "buffer_load_dwordx4 v[52:55], v195, s[8:11], s84 offen offset:1024 sc1\n"
    "v_exp_f32 v120, v104\n"
    "v_exp_f32 v121, v105\n"
    "v_cvt_pk_bf16_f32 v115, v124, v125\n"
    "v_exp_f32 v122, v106\n"
    "v_exp_f32 v123, v107\n"
    "v_cvt_pk_bf16_f32 v116, v120, v121\n"
    "v_mfma_f32_32x32x16_bf16 v[144:159], v[72:75], v[112:115], v[144:159]\n"
    "buffer_load_dwordx4 v205, s[20:23], s37 offen offset:1024 lds\n"
    "v_exp_f32 v124, v108\n"
    "v_exp_f32 v125, v109\n"
    "v_cvt_pk_bf16_f32 v117, v122, v123\n"
    "v_exp_f32 v120, v110\n"
    "v_exp_f32 v121, v111\n"
    "v_cvt_pk_bf16_f32 v118, v124, v125\n"
    "v_mfma_f32_32x32x16_bf16 v[96:111], v[64:67], v[248:251], 0\n"
    "v_exp_f32 v122, v80\n"
    "v_exp_f32 v123, v81\n"
    "v_cvt_pk_bf16_f32 v119, v120, v121\n"
    "v_exp_f32 v124, v82\n"
    "v_exp_f32 v125, v83\n"
    "v_cvt_pk_bf16_f32 v112, v122, v123\n"
    "v_mfma_f32_32x32x16_bf16 v[144:159], v[76:79], v[116:119], v[144:159]\n"
    "buffer_load_dwordx4 v206, s[20:23], s37 offen offset:2048 lds\n"
    "v_exp_f32 v120, v84\n"
    "v_exp_f32 v121, v85\n"
    "v_cvt_pk_bf16_f32 v113, v124, v125\n"
    "v_exp_f32 v122, v86\n"
    "v_exp_f32 v123, v87\n"
    "v_cvt_pk_bf16_f32 v114, v120, v121\n"
    "v_mfma_f32_32x32x16_bf16 v[96:111], v[68:71], v[252:255], v[96:111]\n"
    "buffer_load_dwordx4 v[56:59], v195, s[8:11], s84 offen offset:2048 sc1\n"
    "v_exp_f32 v124, v88\n"
    "v_exp_f32 v125, v89\n"
    "v_cvt_pk_bf16_f32 v115, v122, v123\n"
    "v_exp_f32 v120, v90\n"
    "v_exp_f32 v121, v91\n"
    "v_cvt_pk_bf16_f32 v116, v124, v125\n"
    "v_mfma_f32_32x32x16_bf16 v[160:175], v[72:75], v[112:115], v[160:175]\n"
    "s_waitcnt vmcnt(17)\n"
    "v_exp_f32 v122, v92\n"
    "v_exp_f32 v123, v93\n"
    "v_cvt_pk_bf16_f32 v117, v120, v121\n"
    "v_exp_f32 v124, v94\n"
    "v_exp_f32 v125, v95\n"
    "v_cvt_pk_bf16_f32 v118, v122, v123\n"
    "v_mfma_f32_32x32x16_bf16 v[80:95], v[16:19], v[224:227], 0\n"
    "buffer_load_dwordx4 v207, s[20:23], s37 offen offset:3072 lds\n"
    "v_exp_f32 v120, v96\n"
    "v_exp_f32 v121, v97\n"
    "v_cvt_pk_bf16_f32 v119, v124, v125\n"
    "v_exp_f32 v122, v98\n"
    "v_exp_f32 v123, v99\n"
    "v_cvt_pk_bf16_f32 v112, v120, v121\n"
    "v_mfma_f32_32x32x16_bf16 v[160:175], v[76:79], v[116:119], v[160:175]\n"
    "s_add_u32 m0, s78, 32768\n"
    "v_exp_f32 v124, v100\n"
    "v_exp_f32 v125, v101\n"
    "v_cvt_pk_bf16_f32 v113, v122, v123\n"
    "v_exp_f32 v120, v102\n"
    "v_exp_f32 v121, v103\n"
    "v_cvt_pk_bf16_f32 v114, v124, v125\n"
    "v_mfma_f32_32x32x16_bf16 v[80:95], v[20:23], v[228:231], v[80:95]\n"
    "buffer_load_dwordx4 v[60:63], v195, s[8:11], s84 offen offset:3072 sc1\n"
    "v_exp_f32 v122, v104\n"
    "v_exp_f32 v123, v105\n"
    "v_cvt_pk_bf16_f32 v115, v120, v121\n"
    "v_exp_f32 v124, v106\n"
    "v_exp_f32 v125, v107\n"
    "v_cvt_pk_bf16_f32 v116, v122, v123\n"
    "v_mfma_f32_32x32x16_bf16 v[176:191], v[72:75], v[112:115], v[176:191]\n"
    "buffer_load_dwordx4 v204, s[20:23], s38 offen lds\n"
    "v_exp_f32 v120, v108\n"
    "v_exp_f32 v121, v109\n"
    "v_cvt_pk_bf16_f32 v117, v124, v125\n"
    "v_exp_f32 v122, v110\n"
    "v_exp_f32 v123, v111\n"
    "v_cvt_pk_bf16_f32 v118, v120, v121\n"
    "v_mfma_f32_32x32x16_bf16 v[96:111], v[16:19], v[232:235], 0\n"
    "buffer_load_dwordx4 v205, s[20:23], s38 offen offset:1024 lds\n"
    "v_exp_f32 v124, v80\n"
    "v_exp_f32 v125, v81\n"
    "v_cvt_pk_bf16_f32 v119, v122, v123\n"
    "v_exp_f32 v120, v82\n"
    "v_exp_f32 v121, v83\n"
    "v_cvt_pk_bf16_f32 v112, v124, v125\n"
    "v_mfma_f32_32x32x16_bf16 v[176:191], v[76:79], v[116:119], v[176:191]\n"
    "s_xor_b32 s84, s99, 28672\n"
    "v_exp_f32 v122, v84\n"
    "v_exp_f32 v123, v85\n"
    "v_cvt_pk_bf16_f32 v113, v120, v121\n"
    "v_exp_f32 v124, v86\n"
    "v_exp_f32 v125, v87\n"
    "v_cvt_pk_bf16_f32 v114, v122, v123\n"
    "v_mfma_f32_32x32x16_bf16 v[96:111], v[20:23], v[236:239], v[96:111]\n"
    "buffer_load_dwordx4 v[64:67], v195, s[8:11], s84 offen sc1\n"
    "v_exp_f32 v120, v88\n"
    "v_exp_f32 v121, v89\n"
    "v_cvt_pk_bf16_f32 v115, v124, v125\n"
    "v_exp_f32 v122, v90\n"
    "v_exp_f32 v123, v91\n"
    "v_cvt_pk_bf16_f32 v116, v120, v121\n"
    "v_mfma_f32_32x32x16_bf16 v[128:143], v[24:27], v[112:115], v[128:143]\n"
    "buffer_load_dwordx4 v206, s[20:23], s38 offen offset:2048 lds\n"
    "v_exp_f32 v124, v92\n"
    "v_exp_f32 v125, v93\n"
    "v_cvt_pk_bf16_f32 v117, v122, v123\n"
    "v_exp_f32 v120, v94\n"
    "v_exp_f32 v121, v95\n"
    "v_cvt_pk_bf16_f32 v118, v124, v125\n"
    "v_mfma_f32_32x32x16_bf16 v[80:95], v[16:19], v[240:243], 0\n"
    "buffer_load_dword v220, v218, s[8:11], s53 offen sc1\n"
    "v_exp_f32 v122, v96\n"
    "v_exp_f32 v123, v97\n"
    "v_cvt_pk_bf16_f32 v119, v120, v121\n"
    "v_exp_f32 v124, v98\n"
    "v_exp_f32 v125, v99\n"
    "v_cvt_pk_bf16_f32 v112, v122, v123\n"
    "v_mfma_f32_32x32x16_bf16 v[128:143], v[28:31], v[116:119], v[128:143]\n"
    "v_exp_f32 v120, v100\n"
    "v_exp_f32 v121, v101\n"
    "v_cvt_pk_bf16_f32 v113, v124, v125\n"
    "v_exp_f32 v122, v102\n"
    "v_exp_f32 v123, v103\n"
    "v_cvt_pk_bf16_f32 v114, v120, v121\n"
    "v_mfma_f32_32x32x16_bf16 v[80:95], v[20:23], v[244:247], v[80:95]\n"
    "buffer_load_dwordx4 v[68:71], v195, s[8:11], s84 offen offset:1024 sc1\n"
    "v_exp_f32 v124, v104\n"
    "v_exp_f32 v125, v105\n"
    "v_cvt_pk_bf16_f32 v115, v122, v123\n"
    "v_exp_f32 v120, v106\n"
    "v_exp_f32 v121, v107\n"
    "v_cvt_pk_bf16_f32 v116, v124, v125\n"
    "v_mfma_f32_32x32x16_bf16 v[144:159], v[24:27], v[112:115], v[144:159]\n"
    "buffer_load_dwordx4 v207, s[20:23], s38 offen offset:3072 lds\n"
    "v_exp_f32 v122, v108\n"
    "v_exp_f32 v123, v109\n"
    "v_cvt_pk_bf16_f32 v117, v120, v121\n"
    "v_exp_f32 v124, v110\n"
    "v_exp_f32 v125, v111\n"
    "v_cvt_pk_bf16_f32 v118, v122, v123\n"
    "v_mfma_f32_32x32x16_bf16 v[96:111], v[16:19], v[248:251], 0\n"
    "s_add_u32 s38, s38, 28672\n"
    "v_exp_f32 v120, v80\n"
    "v_exp_f32 v121, v81\n"
    "v_cvt_pk_bf16_f32 v119, v124, v125\n"
    "v_exp_f32 v122, v82\n"
    "v_exp_f32 v123, v83\n"
    "v_cvt_pk_bf16_f32 v112, v120, v121\n"
    "v_mfma_f32_32x32x16_bf16 v[144:159], v[28:31], v[116:119], v[144:159]\n"
    "s_add_u32 m0, s78, 0\n"
    "v_exp_f32 v124, v84\n"
    "v_exp_f32 v125, v85\n"
    "v_cvt_pk_bf16_f32 v113, v122, v123\n"
    "v_exp_f32 v120, v86\n"
    "v_exp_f32 v121, v87\n"
    "v_cvt_pk_bf16_f32 v114, v124, v125\n"
    "v_mfma_f32_32x32x16_bf16 v[96:111], v[20:23], v[252:255], v[96:111]\n"
    "buffer_load_dwordx4 v[72:75], v195, s[8:11], s84 offen offset:2048 sc1\n"
    "v_exp_f32 v122, v88\n"
    "v_exp_f32 v123, v89\n"
    "v_cvt_pk_bf16_f32 v115, v120, v121\n"
    "v_exp_f32 v124, v90\n"
    "v_exp_f32 v125, v91\n"
    "v_cvt_pk_bf16_f32 v116, v122, v123\n"
    "v_mfma_f32_32x32x16_bf16 v[160:175], v[24:27], v[112:115], v[160:175]\n"
    "s_waitcnt vmcnt(17)\n"
    "v_exp_f32 v120, v92\n"
    "v_exp_f32 v121, v93\n"
    "v_cvt_pk_bf16_f32 v117, v124, v125\n"
    "v_exp_f32 v122, v94\n"
    "v_exp_f32 v123, v95\n"
    "v_cvt_pk_bf16_f32 v118, v120, v121\n"
    "v_mfma_f32_32x32x16_bf16 v[80:95], v[32:35], v[224:227], 0\n"
    "buffer_load_dwordx4 v204, s[20:23], s38 offen lds\n"
    "v_exp_f32 v124, v96\n"
    "v_exp_f32 v125, v97\n"
    "v_cvt_pk_bf16_f32 v119, v122, v123\n"
    "v_exp_f32 v120, v98\n"
    "v_exp_f32 v121, v99\n"
    "v_cvt_pk_bf16_f32 v112, v124, v125\n"
    "v_mfma_f32_32x32x16_bf16 v[160:175], v[28:31], v[116:119], v[160:175]\n"
    "buffer_load_dwordx4 v205, s[20:23], s38 offen offset:1024 lds\n"
    "v_exp_f32 v122, v100\n"
    "v_exp_f32 v123, v101\n"
    "v_cvt_pk_bf16_f32 v113, v120, v121\n"
    "v_exp_f32 v124, v102\n"
    "v_exp_f32 v125, v103\n"
    "v_cvt_pk_bf16_f32 v114, v122, v123\n"
    "v_mfma_f32_32x32x16_bf16 v[80:95], v[36:39], v[228:231], v[80:95]\n"
    "buffer_load_dwordx4 v[76:79], v195, s[8:11], s84 offen offset:3072 sc1\n"
    "v_exp_f32 v120, v104\n"
    "v_exp_f32 v121, v105\n"
    "v_cvt_pk_bf16_f32 v115, v124, v125\n"
    "v_exp_f32 v122, v106\n"
    "v_exp_f32 v123, v107\n"
    "v_cvt_pk_bf16_f32 v116, v120, v121\n"
    "v_mfma_f32_32x32x16_bf16 v[176:191], v[24:27], v[112:115], v[176:191]\n"
    "v_exp_f32 v124, v108\n"
    "v_exp_f32 v125, v109\n"
    "v_cvt_pk_bf16_f32 v117, v122, v123\n"
    "v_exp_f32 v120, v110\n"
    "v_exp_f32 v121, v111\n"
    "v_cvt_pk_bf16_f32 v118, v124, v125\n"
    "v_mfma_f32_32x32x16_bf16 v[96:111], v[32:35], v[232:235], 0\n"
    "buffer_load_dwordx4 v206, s[20:23], s38 offen offset:2048 lds\n"
    "v_exp_f32 v122, v80\n"
    "v_exp_f32 v123, v81\n"
    "v_cvt_pk_bf16_f32 v119, v120, v121\n"
    "v_exp_f32 v124, v82\n"
    "v_exp_f32 v125, v83\n"
    "v_cvt_pk_bf16_f32 v112, v122, v123\n"
    "v_mfma_f32_32x32x16_bf16 v[176:191], v[28:31], v[116:119], v[176:191]\n"
    "s_xor_b32 s84, s99, 32768\n"
    "v_exp_f32 v120, v84\n"
    "v_exp_f32 v121, v85\n"
    "v_cvt_pk_bf16_f32 v113, v124, v125\n"
    "v_exp_f32 v122, v86\n"
    "v_exp_f32 v123, v87\n"
    "v_cvt_pk_bf16_f32 v114, v120, v121\n"
    "v_mfma_f32_32x32x16_bf16 v[96:111], v[36:39], v[236:239], v[96:111]\n"
    "buffer_load_dwordx4 v[16:19], v195, s[8:11], s84 offen sc1\n"
    "v_exp_f32 v124, v88\n"
    "v_exp_f32 v125, v89\n"
    "v_cvt_pk_bf16_f32 v115, v122, v123\n"
    "v_exp_f32 v120, v90\n"
    "v_exp_f32 v121, v91\n"
    "v_cvt_pk_bf16_f32 v116, v124, v125\n"
    "v_mfma_f32_32x32x16_bf16 v[128:143], v[40:43], v[112:115], v[128:143]\n"
    "buffer_load_dwordx4 v207, s[20:23], s38 offen offset:3072 lds\n"
    "v_exp_f32 v122, v92\n"
    "v_exp_f32 v123, v93\n"
    "v_cvt_pk_bf16_f32 v117, v120, v121\n"
    "v_exp_f32 v124, v94\n"
    "v_exp_f32 v125, v95\n"
    "v_cvt_pk_bf16_f32 v118, v122, v123\n"
    "v_mfma_f32_32x32x16_bf16 v[80:95], v[32:35], v[240:243], 0\n"
    "s_add_u32 m0, s78, 4096\n"
    "v_exp_f32 v120, v96\n"
    "v_exp_f32 v121, v97\n"
    "v_cvt_pk_bf16_f32 v119, v124, v125\n"
    "v_exp_f32 v122, v98\n"
    "v_exp_f32 v123, v99\n"
    "v_cvt_pk_bf16_f32 v112, v120, v121\n"
    "v_mfma_f32_32x32x16_bf16 v[128:143], v[44:47], v[116:119], v[128:143]\n"
    "buffer_load_dwordx4 v204, s[20:23], s39 offen lds\n"
    "v_exp_f32 v124, v100\n"
    "v_exp_f32 v125, v101\n"
    "v_cvt_pk_bf16_f32 v113, v122, v123\n"
    "v_exp_f32 v120, v102\n"
    "v_exp_f32 v121, v103\n"
    "v_cvt_pk_bf16_f32 v114, v124, v125\n"
    "v_mfma_f32_32x32x16_bf16 v[80:95], v[36:39], v[244:247], v[80:95]\n"
    "buffer_load_dwordx4 v[20:23], v195, s[8:11], s84 offen offset:1024 sc1\n"
    "v_exp_f32 v122, v104\n"
    "v_exp_f32 v123, v105\n"
    "v_cvt_pk_bf16_f32 v115, v120, v121\n"
    "v_exp_f32 v124, v106\n"
    "v_exp_f32 v125, v107\n"
    "v_cvt_pk_bf16_f32 v116, v122, v123\n"
    "v_mfma_f32_32x32x16_bf16 v[144:159], v[40:43], v[112:115], v[144:159]\n"
    "buffer_load_dwordx4 v205, s[20:23], s39 offen offset:1024 lds\n"
    "v_exp_f32 v120, v108\n"
    "v_exp_f32 v121, v109\n"
    "v_cvt_pk_bf16_f32 v117, v124, v125\n"
    "v_exp_f32 v122, v110\n"
    "v_exp_f32 v123, v111\n"
    "v_cvt_pk_bf16_f32 v118, v120, v121\n"
    "v_mfma_f32_32x32x16_bf16 v[96:111], v[32:35], v[248:251], 0\n"
    "s_waitcnt vmcnt(12)\n"
    "v_exp_f32 v124, v80\n"
    "v_exp_f32 v125, v81\n"
    "v_cvt_pk_bf16_f32 v119, v122, v123\n"
    "v_exp_f32 v120, v82\n"
    "v_exp_f32 v121, v83\n"
    "v_cvt_pk_bf16_f32 v112, v124, v125\n"
    "v_mfma_f32_32x32x16_bf16 v[144:159], v[44:47], v[116:119], v[144:159]\n"
    "v_cmp_ne_u32 vcc, 1, v220\n"
    "v_exp_f32 v122, v84\n"
    "v_exp_f32 v123, v85\n"
    "v_cvt_pk_bf16_f32 v113, v120, v121\n"
    "v_exp_f32 v124, v86\n"
    "v_exp_f32 v125, v87\n"
    "v_cvt_pk_bf16_f32 v114, v122, v123\n"
    "v_mfma_f32_32x32x16_bf16 v[96:111], v[36:39], v[252:255], v[96:111]\n"
    "buffer_load_dwordx4 v[24:27], v195, s[8:11], s84 offen offset:2048 sc1\n"
    "v_exp_f32 v120, v88\n"
    "v_exp_f32 v121, v89\n"
    "v_cvt_pk_bf16_f32 v115, v124, v125\n"
    "v_exp_f32 v122, v90\n"
    "v_exp_f32 v123, v91\n"
    "v_cvt_pk_bf16_f32 v116, v120, v121\n"
    "v_mfma_f32_32x32x16_bf16 v[160:175], v[40:43], v[112:115], v[160:175]\n"
    "v_exp_f32 v124, v92\n"
    "v_exp_f32 v125, v93\n"
    "v_cvt_pk_bf16_f32 v117, v122, v123\n"
    "v_exp_f32 v120, v94\n"
    "v_exp_f32 v121, v95\n"
    "v_cvt_pk_bf16_f32 v118, v124, v125\n"
    "v_mfma_f32_32x32x16_bf16 v[80:95], v[48:51], v[224:227], 0\n"
    "s_cbranch_vccnz .Lo_29_%=\n"
    ".Lr_30_%=:\n"
    "v_exp_f32 v122, v96\n"
    "v_exp_f32 v123, v97\n"
    "v_cvt_pk_bf16_f32 v119, v120, v121\n"
    "v_exp_f32 v124, v98\n"
    "v_exp_f32 v125, v99\n"
    "v_cvt_pk_bf16_f32 v112, v122, v123\n"
    "v_mfma_f32_32x32x16_bf16 v[160:175], v[44:47], v[116:119], v[160:175]\n"
    "buffer_load_dwordx4 a[0:3], v10, s[32:35], s43 offen sc1\n"
    "v_exp_f32 v120, v100\n"
    "v_exp_f32 v121, v101\n"
    "v_cvt_pk_bf16_f32 v113, v124, v125\n"
    "v_exp_f32 v122, v102\n"
    "v_exp_f32 v123, v103\n"
    "v_cvt_pk_bf16_f32 v114, v120, v121\n"
    "v_mfma_f32_32x32x16_bf16 v[80:95], v[52:55], v[228:231], v[80:95]\n"
    "buffer_load_dwordx4 v[28:31], v195, s[8:11], s84 offen offset:3072 sc1\n"
    "v_exp_f32 v124, v104\n"
    "v_exp_f32 v125, v105\n"
    "v_cvt_pk_bf16_f32 v115, v122, v123\n"
    "v_exp_f32 v120, v106\n"
    "v_exp_f32 v121, v107\n"
    "v_cvt_pk_bf16_f32 v116, v124, v125\n"
    "v_mfma_f32_32x32x16_bf16 v[176:191], v[40:43], v[112:115], v[176:191]\n"
    "buffer_load_dwordx4 a[4:7], v11, s[32:35], s43 offen sc1\n"
    "v_exp_f32 v122, v108\n"
    "v_exp_f32 v123, v109\n"
    "v_cvt_pk_bf16_f32 v117, v120, v121\n"
    "v_exp_f32 v124, v110\n"
    "v_exp_f32 v125, v111\n"
    "v_cvt_pk_bf16_f32 v118, v122, v123\n"
    "v_mfma_f32_32x32x16_bf16 v[96:111], v[48:51], v[232:235], 0\n"
    "v_exp_f32 v120, v80\n"
    "v_exp_f32 v121, v81\n"
    "v_cvt_pk_bf16_f32 v119, v124, v125\n"
    "v_exp_f32 v122, v82\n"
    "v_exp_f32 v123, v83\n"
    "v_cvt_pk_bf16_f32 v112, v120, v121\n"
    "v_mfma_f32_32x32x16_bf16 v[176:191], v[44:47], v[116:119], v[176:191]\n"
    "s_xor_b32 s84, s99, 36864\n"
    "v_exp_f32 v124, v84\n"
    "v_exp_f32 v125, v85\n"
    "v_cvt_pk_bf16_f32 v113, v122, v123\n"
    "v_exp_f32 v120, v86\n"
    "v_exp_f32 v121, v87\n"
    "v_cvt_pk_bf16_f32 v114, v124, v125\n"
    "v_mfma_f32_32x32x16_bf16 v[96:111], v[52:55], v[236:239], v[96:111]\n"
    "buffer_load_dwordx4 v[32:35], v195, s[8:11], s84 offen sc1\n"
    "v_exp_f32 v122, v88\n"
    "v_exp_f32 v123, v89\n"
    "v_cvt_pk_bf16_f32 v115, v120, v121\n"
    "v_exp_f32 v124, v90\n"
    "v_exp_f32 v125, v91\n"
    "v_cvt_pk_bf16_f32 v116, v122, v123\n"
    "v_mfma_f32_32x32x16_bf16 v[128:143], v[56:59], v[112:115], v[128:143]\n"
    "buffer_load_dwordx4 a[8:11], v12, s[32:35], s43 offen sc1\n"
    "v_exp_f32 v120, v92\n"
    "v_exp_f32 v121, v93\n"
    "v_cvt_pk_bf16_f32 v117, v124, v125\n"
    "v_exp_f32 v122, v94\n"
    "v_exp_f32 v123, v95\n"
    "v_cvt_pk_bf16_f32 v118, v120, v121\n"
    "v_mfma_f32_32x32x16_bf16 v[80:95], v[48:51], v[240:243], 0\n"
    "buffer_load_dwordx4 a[12:15], v13, s[32:35], s43 offen sc1\n"
    "v_exp_f32 v124, v96\n"
    "v_exp_f32 v125, v97\n"
    "v_cvt_pk_bf16_f32 v119, v122, v123\n"
    "v_exp_f32 v120, v98\n"
    "v_exp_f32 v121, v99\n"
    "v_cvt_pk_bf16_f32 v112, v124, v125\n"
    "v_mfma_f32_32x32x16_bf16 v[128:143], v[60:63], v[116:119], v[128:143]\n"
    ".Lpage_5_%=:\n"
    "s_cbranch_execz .Lpage_0_%=\n"
    "ds_read_b64_tr_b16 a[224:225], v208 offset:20480\n"
    "v_exp_f32 v122, v100\n"
    "v_exp_f32 v123, v101\n"
    "v_cvt_pk_bf16_f32 v113, v120, v121\n"
    "v_exp_f32 v124, v102\n"
    "v_exp_f32 v125, v103\n"
    "v_cvt_pk_bf16_f32 v114, v122, v123\n"
    "v_mfma_f32_32x32x16_bf16 v[80:95], v[52:55], v[244:247], v[80:95]\n"
    "buffer_load_dwordx4 v[36:39], v195, s[8:11], s84 offen offset:1024 sc1\n"
    "v_exp_f32 v120, v104\n"
    "v_exp_f32 v121, v105\n"
    "v_cvt_pk_bf16_f32 v115, v124, v125\n"
    "v_exp_f32 v122, v106\n"
    "v_exp_f32 v123, v107\n"
    "v_cvt_pk_bf16_f32 v116, v120, v121\n"
    "v_mfma_f32_32x32x16_bf16 v[144:159], v[56:59], v[112:115], v[144:159]\n"
    "buffer_load_dwordx4 a[16:19], v10, s[32:35], s43 offen offset:1024 sc1\n"
    "v_exp_f32 v124, v108\n"
    "v_exp_f32 v125, v109\n"
    "v_cvt_pk_bf16_f32 v117, v122, v123\n"
    "v_exp_f32 v120, v110\n"
    "v_exp_f32 v121, v111\n"
    "v_cvt_pk_bf16_f32 v118, v124, v125\n"
    "v_mfma_f32_32x32x16_bf16 v[96:111], v[48:51], v[248:251], 0\n"
    "ds_read_b64_tr_b16 a[226:227], v208 offset:22528\n"
    "v_exp_f32 v122, v80\n"
    "v_exp_f32 v123, v81\n"
    "v_cvt_pk_bf16_f32 v119, v120, v121\n"
    "v_exp_f32 v124, v82\n"
    "v_exp_f32 v125, v83\n"
    "v_cvt_pk_bf16_f32 v112, v122, v123\n"
    "v_mfma_f32_32x32x16_bf16 v[144:159], v[60:63], v[116:119], v[144:159]\n"
    "buffer_load_dwordx4 a[20:23], v11, s[32:35], s43 offen offset:1024 sc1\n"
    "v_exp_f32 v120, v84\n"
    "v_exp_f32 v121, v85\n"
    "v_cvt_pk_bf16_f32 v113, v124, v125\n"
    "v_exp_f32 v122, v86\n"
    "v_exp_f32 v123, v87\n"
    "v_cvt_pk_bf16_f32 v114, v120, v121\n"
    "v_mfma_f32_32x32x16_bf16 v[96:111], v[52:55], v[252:255], v[96:111]\n"
    "buffer_load_dwordx4 v[40:43], v195, s[8:11], s84 offen offset:2048 sc1\n"
    "v_exp_f32 v124, v88\n"
    "v_exp_f32 v125, v89\n"
    "v_cvt_pk_bf16_f32 v115, v122, v123\n"
    "v_exp_f32 v120, v90\n"
    "v_exp_f32 v121, v91\n"
    "v_cvt_pk_bf16_f32 v116, v124, v125\n"
    "v_mfma_f32_32x32x16_bf16 v[160:175], v[56:59], v[112:115], v[160:175]\n"
    "s_waitcnt vmcnt(17)\n"
    "v_exp_f32 v122, v92\n"
    "v_exp_f32 v123, v93\n"
    "v_cvt_pk_bf16_f32 v117, v120, v121\n"
    "v_exp_f32 v124, v94\n"
    "v_exp_f32 v125, v95\n"
    "v_cvt_pk_bf16_f32 v118, v122, v123\n"
    "v_mfma_f32_32x32x16_bf16 v[80:95], v[64:67], v[224:227], 0\n"
    "buffer_load_dwordx4 a[24:27], v12, s[32:35], s43 offen offset:1024 sc1\n"
    "v_exp_f32 v120, v96\n"
    "v_exp_f32 v121, v97\n"
    "v_cvt_pk_bf16_f32 v119, v124, v125\n"
    "v_exp_f32 v122, v98\n"
    "v_exp_f32 v123, v99\n"
    "v_cvt_pk_bf16_f32 v112, v120, v121\n"
    "v_mfma_f32_32x32x16_bf16 v[160:175], v[60:63], v[116:119], v[160:175]\n"
    "ds_read_b64_tr_b16 a[228:229], v208 offset:24576\n"
    "v_exp_f32 v124, v100\n"
    "v_exp_f32 v125, v101\n"
    "v_cvt_pk_bf16_f32 v113, v122, v123\n"
    "v_exp_f32 v120, v102\n"
    "v_exp_f32 v121, v103\n"
    "v_cvt_pk_bf16_f32 v114, v124, v125\n"
    "v_mfma_f32_32x32x16_bf16 v[80:95], v[68:71], v[228:231], v[80:95]\n"
    "buffer_load_dwordx4 v[44:47], v195, s[8:11], s84 offen offset:3072 sc1\n"
    "v_exp_f32 v122, v104\n"
    "v_exp_f32 v123, v105\n"
    "v_cvt_pk_bf16_f32 v115, v120, v121\n"
    "v_exp_f32 v124, v106\n"
    "v_exp_f32 v125, v107\n"
    "v_cvt_pk_bf16_f32 v116, v122, v123\n"
    "v_mfma_f32_32x32x16_bf16 v[176:191], v[56:59], v[112:115], v[176:191]\n"
    "buffer_load_dwordx4 a[28:31], v13, s[32:35], s43 offen offset:1024 sc1\n"
    "v_exp_f32 v120, v108\n"
    "v_exp_f32 v121, v109\n"
    "v_cvt_pk_bf16_f32 v117, v124, v125\n"
    "v_exp_f32 v122, v110\n"
    "v_exp_f32 v123, v111\n"
    "v_cvt_pk_bf16_f32 v118, v120, v121\n"
    "v_mfma_f32_32x32x16_bf16 v[96:111], v[64:67], v[232:235], 0\n"
    "buffer_load_dwordx4 v206, s[20:23], s39 offen offset:2048 lds\n"
    "v_exp_f32 v124, v80\n"
    "v_exp_f32 v125, v81\n"
    "v_cvt_pk_bf16_f32 v119, v122, v123\n"
    "v_exp_f32 v120, v82\n"
    "v_exp_f32 v121, v83\n"
    "v_cvt_pk_bf16_f32 v112, v124, v125\n"
    "v_mfma_f32_32x32x16_bf16 v[176:191], v[60:63], v[116:119], v[176:191]\n"
    "s_xor_b32 s84, s99, 40960\n"
    "v_exp_f32 v122, v84\n"
    "v_exp_f32 v123, v85\n"
    "v_cvt_pk_bf16_f32 v113, v120, v121\n"
    "v_exp_f32 v124, v86\n"
    "v_exp_f32 v125, v87\n"
    "v_cvt_pk_bf16_f32 v114, v122, v123\n"
    "v_mfma_f32_32x32x16_bf16 v[96:111], v[68:71], v[236:239], v[96:111]\n"
    "buffer_load_dwordx4 v[48:51], v195, s[8:11], s84 offen sc1\n"
    "v_exp_f32 v120, v88\n"
    "v_exp_f32 v121, v89\n"
    "v_cvt_pk_bf16_f32 v115, v124, v125\n"
    "v_exp_f32 v122, v90\n"
    "v_exp_f32 v123, v91\n"
    "v_cvt_pk_bf16_f32 v116, v120, v121\n"
    "v_mfma_f32_32x32x16_bf16 v[128:143], v[72:75], v[112:115], v[128:143]\n"
    "buffer_load_dwordx4 a[32:35], v10, s[32:35], s44 offen sc1\n"
    "v_exp_f32 v124, v92\n"
    "v_exp_f32 v125, v93\n"
    "v_cvt_pk_bf16_f32 v117, v122, v123\n"
    "v_exp_f32 v120, v94\n"
    "v_exp_f32 v121, v95\n"
    "v_cvt_pk_bf16_f32 v118, v124, v125\n"
    "v_mfma_f32_32x32x16_bf16 v[80:95], v[64:67], v[240:243], 0\n"
    "buffer_load_dwordx4 a[36:39], v11, s[32:35], s44 offen sc1\n"
    "v_exp_f32 v122, v96\n"
    "v_exp_f32 v123, v97\n"
    "v_cvt_pk_bf16_f32 v119, v120, v121\n"
    "v_exp_f32 v124, v98\n"
    "v_exp_f32 v125, v99\n"
    "v_cvt_pk_bf16_f32 v112, v122, v123\n"
    "v_mfma_f32_32x32x16_bf16 v[128:143], v[76:79], v[116:119], v[128:143]\n"
    "ds_read_b64_tr_b16 a[230:231], v208 offset:26624\n"
    "v_exp_f32 v120, v100\n"
    "v_exp_f32 v121, v101\n"
    "v_cvt_pk_bf16_f32 v113, v124, v125\n"
    "v_exp_f32 v122, v102\n"
    "v_exp_f32 v123, v103\n"
    "v_cvt_pk_bf16_f32 v114, v120, v121\n"
    "v_mfma_f32_32x32x16_bf16 v[80:95], v[68:71], v[244:247], v[80:95]\n"
    "buffer_load_dwordx4 v[52:55], v195, s[8:11], s84 offen offset:1024 sc1\n"
    "v_exp_f32 v124, v104\n"
    "v_exp_f32 v125, v105\n"
    "v_cvt_pk_bf16_f32 v115, v122, v123\n"
    "v_exp_f32 v120, v106\n"
    "v_exp_f32 v121, v107\n"
    "v_cvt_pk_bf16_f32 v116, v124, v125\n"
    "v_mfma_f32_32x32x16_bf16 v[144:159], v[72:75], v[112:115], v[144:159]\n"
    "buffer_load_dwordx4 a[40:43], v12, s[32:35], s44 offen sc1\n"
    "v_exp_f32 v122, v108\n"
    "v_exp_f32 v123, v109\n"
    "v_cvt_pk_bf16_f32 v117, v120, v121\n"
    "v_exp_f32 v124, v110\n"
    "v_exp_f32 v125, v111\n"
    "v_cvt_pk_bf16_f32 v118, v122, v123\n"
    "v_mfma_f32_32x32x16_bf16 v[96:111], v[64:67], v[248:251], 0\n"
    "ds_read_b64_tr_b16 a[232:233], v209 offset:20480\n"
    "v_exp_f32 v120, v80\n"
    "v_exp_f32 v121, v81\n"
    "v_cvt_pk_bf16_f32 v119, v124, v125\n"
    "v_exp_f32 v122, v82\n"
    "v_exp_f32 v123, v83\n"
    "v_cvt_pk_bf16_f32 v112, v120, v121\n"
    "v_mfma_f32_32x32x16_bf16 v[144:159], v[76:79], v[116:119], v[144:159]\n"
    "buffer_load_dwordx4 a[44:47], v13, s[32:35], s44 offen sc1\n"
    "v_exp_f32 v124, v84\n"
    "v_exp_f32 v125, v85\n"
    "v_cvt_pk_bf16_f32 v113, v122, v123\n"
    "v_exp_f32 v120, v86\n"
    "v_exp_f32 v121, v87\n"
    "v_cvt_pk_bf16_f32 v114, v124, v125\n"
    "v_mfma_f32_32x32x16_bf16 v[96:111], v[68:71], v[252:255], v[96:111]\n"
    "buffer_load_dwordx4 v[56:59], v195, s[8:11], s84 offen offset:2048 sc1\n"
    "v_exp_f32 v122, v88\n"
    "v_exp_f32 v123, v89\n"
    "v_cvt_pk_bf16_f32 v115, v120, v121\n"
    "v_exp_f32 v124, v90\n"
    "v_exp_f32 v125, v91\n"
    "v_cvt_pk_bf16_f32 v116, v122, v123\n"
    "v_mfma_f32_32x32x16_bf16 v[160:175], v[72:75], v[112:115], v[160:175]\n"
    "s_waitcnt vmcnt(19)\n"
    "v_exp_f32 v120, v92\n"
    "v_exp_f32 v121, v93\n"
    "v_cvt_pk_bf16_f32 v117, v124, v125\n"
    "v_exp_f32 v122, v94\n"
    "v_exp_f32 v123, v95\n"
    "v_cvt_pk_bf16_f32 v118, v120, v121\n"
    "v_mfma_f32_32x32x16_bf16 v[80:95], v[16:19], v[224:227], 0\n"
    "buffer_load_dwordx4 a[48:51], v10, s[32:35], s44 offen offset:1024 sc1\n"
    "v_exp_f32 v124, v96\n"
    "v_exp_f32 v125, v97\n"
    "v_cvt_pk_bf16_f32 v119, v122, v123\n"
    "v_exp_f32 v120, v98\n"
    "v_exp_f32 v121, v99\n"
    "v_cvt_pk_bf16_f32 v112, v124, v125\n"
    "v_mfma_f32_32x32x16_bf16 v[160:175], v[76:79], v[116:119], v[160:175]\n"
    "ds_read_b64_tr_b16 a[234:235], v209 offset:22528\n"
    "v_exp_f32 v122, v100\n"
    "v_exp_f32 v123, v101\n"
    "v_cvt_pk_bf16_f32 v113, v120, v121\n"
    "v_exp_f32 v124, v102\n"
    "v_exp_f32 v125, v103\n"
    "v_cvt_pk_bf16_f32 v114, v122, v123\n"
    "v_mfma_f32_32x32x16_bf16 v[80:95], v[20:23], v[228:231], v[80:95]\n"
    "buffer_load_dwordx4 v[60:63], v195, s[8:11], s84 offen offset:3072 sc1\n"
    "v_exp_f32 v120, v104\n"
    "v_exp_f32 v121, v105\n"
    "v_cvt_pk_bf16_f32 v115, v124, v125\n"
    "v_exp_f32 v122, v106\n"
    "v_exp_f32 v123, v107\n"
    "v_cvt_pk_bf16_f32 v116, v120, v121\n"
    "v_mfma_f32_32x32x16_bf16 v[176:191], v[72:75], v[112:115], v[176:191]\n"
    "buffer_load_dwordx4 a[52:55], v11, s[32:35], s44 offen offset:1024 sc1\n"
    "v_exp_f32 v124, v108\n"
    "v_exp_f32 v125, v109\n"
    "v_cvt_pk_bf16_f32 v117, v122, v123\n"
    "v_exp_f32 v120, v110\n"
    "v_exp_f32 v121, v111\n"
    "v_cvt_pk_bf16_f32 v118, v124, v125\n"
    "v_mfma_f32_32x32x16_bf16 v[96:111], v[16:19], v[232:235], 0\n"
    "buffer_load_dwordx4 a[56:59], v12, s[32:35], s44 offen offset:1024 sc1\n"
    "v_exp_f32 v122, v80\n"
    "v_exp_f32 v123, v81\n"
    "v_cvt_pk_bf16_f32 v119, v120, v121\n"
    "v_exp_f32 v124, v82\n"
    "v_exp_f32 v125, v83\n"
    "v_cvt_pk_bf16_f32 v112, v122, v123\n"
    "v_mfma_f32_32x32x16_bf16 v[176:191], v[76:79], v[116:119], v[176:191]\n"
    "s_xor_b32 s84, s99, 45056\n"
    "v_exp_f32 v120, v84\n"
    "v_exp_f32 v121, v85\n"
    "v_cvt_pk_bf16_f32 v113, v124, v125\n"
    "v_exp_f32 v122, v86\n"
    "v_exp_f32 v123, v87\n"
    "v_cvt_pk_bf16_f32 v114, v120, v121\n"
    "v_mfma_f32_32x32x16_bf16 v[96:111], v[20:23], v[236:239], v[96:111]\n"
    "buffer_load_dwordx4 v[64:67], v195, s[8:11], s84 offen sc1\n"
    "v_exp_f32 v124, v88\n"
    "v_exp_f32 v125, v89\n"
    "v_cvt_pk_bf16_f32 v115, v122, v123\n"
    "v_exp_f32 v120, v90\n"
    "v_exp_f32 v121, v91\n"
    "v_cvt_pk_bf16_f32 v116, v124, v125\n"
    "v_mfma_f32_32x32x16_bf16 v[128:143], v[24:27], v[112:115], v[128:143]\n"
    "buffer_load_dwordx4 a[60:63], v13, s[32:35], s44 offen offset:1024 sc1\n"
    "v_exp_f32 v122, v92\n"
    "v_exp_f32 v123, v93\n"
    "v_cvt_pk_bf16_f32 v117, v120, v121\n"
    "v_exp_f32 v124, v94\n"
    "v_exp_f32 v125, v95\n"
    "v_cvt_pk_bf16_f32 v118, v122, v123\n"
    "v_mfma_f32_32x32x16_bf16 v[80:95], v[16:19], v[240:243], 0\n"
    "ds_read_b64_tr_b16 a[236:237], v209 offset:24576\n"
    "v_exp_f32 v120, v96\n"
    "v_exp_f32 v121, v97\n"
    "v_cvt_pk_bf16_f32 v119, v124, v125\n"
    "v_exp_f32 v122, v98\n"
    "v_exp_f32 v123, v99\n"
    "v_cvt_pk_bf16_f32 v112, v120, v121\n"
    "v_mfma_f32_32x32x16_bf16 v[128:143], v[28:31], v[116:119], v[128:143]\n"
    "buffer_load_dwordx4 a[64:67], v10, s[32:35], s45 offen sc1\n"
    "v_exp_f32 v124, v100\n"
    "v_exp_f32 v125, v101\n"
    "v_cvt_pk_bf16_f32 v113, v122, v123\n"
    "v_exp_f32 v120, v102\n"
    "v_exp_f32 v121, v103\n"
    "v_cvt_pk_bf16_f32 v114, v124, v125\n"
    "v_mfma_f32_32x32x16_bf16 v[80:95], v[20:23], v[244:247], v[80:95]\n"
    "buffer_load_dwordx4 v[68:71], v195, s[8:11], s84 offen offset:1024 sc1\n"
    "v_exp_f32 v122, v104\n"
    "v_exp_f32 v123, v105\n"
    "v_cvt_pk_bf16_f32 v115, v120, v121\n"
    "v_exp_f32 v124, v106\n"
    "v_exp_f32 v125, v107\n"
    "v_cvt_pk_bf16_f32 v116, v122, v123\n"
    "v_mfma_f32_32x32x16_bf16 v[144:159], v[24:27], v[112:115], v[144:159]\n"
    "ds_read_b64_tr_b16 a[238:239], v209 offset:26624\n"
    "v_exp_f32 v120, v108\n"
    "v_exp_f32 v121, v109\n"
    "v_cvt_pk_bf16_f32 v117, v124, v125\n"
    "v_exp_f32 v122, v110\n"
    "v_exp_f32 v123, v111\n"
    "v_cvt_pk_bf16_f32 v118, v120, v121\n"
    "v_mfma_f32_32x32x16_bf16 v[96:111], v[16:19], v[248:251], 0\n"
    "buffer_load_dwordx4 a[68:71], v11, s[32:35], s45 offen sc1\n"
    "v_exp_f32 v124, v80\n"
    "v_exp_f32 v125, v81\n"
    "v_cvt_pk_bf16_f32 v119, v122, v123\n"
    "v_exp_f32 v120, v82\n"
    "v_exp_f32 v121, v83\n"
    "v_cvt_pk_bf16_f32 v112, v124, v125\n"
    "v_mfma_f32_32x32x16_bf16 v[144:159], v[28:31], v[116:119], v[144:159]\n"
    "buffer_load_dwordx4 v207, s[20:23], s39 offen offset:3072 lds\n"
    "v_exp_f32 v122, v84\n"
    "v_exp_f32 v123, v85\n"
    "v_cvt_pk_bf16_f32 v113, v120, v121\n"
    "v_exp_f32 v124, v86\n"
    "v_exp_f32 v125, v87\n"
    "v_cvt_pk_bf16_f32 v114, v122, v123\n"
    "v_mfma_f32_32x32x16_bf16 v[96:111], v[20:23], v[252:255], v[96:111]\n"
    "buffer_load_dwordx4 v[72:75], v195, s[8:11], s84 offen offset:2048 sc1\n"
    "v_exp_f32 v120, v88\n"
    "v_exp_f32 v121, v89\n"
    "v_cvt_pk_bf16_f32 v115, v124, v125\n"
    "v_exp_f32 v122, v90\n"
    "v_exp_f32 v123, v91\n"
    "v_cvt_pk_bf16_f32 v116, v120, v121\n"
    "v_mfma_f32_32x32x16_bf16 v[160:175], v[24:27], v[112:115], v[160:175]\n"
    "s_waitcnt vmcnt(20)\n"
    "v_exp_f32 v124, v92\n"
    "v_exp_f32 v125, v93\n"
    "v_cvt_pk_bf16_f32 v117, v122, v123\n"
    "v_exp_f32 v120, v94\n"
    "v_exp_f32 v121, v95\n"
    "v_cvt_pk_bf16_f32 v118, v124, v125\n"
    "v_mfma_f32_32x32x16_bf16 v[80:95], v[32:35], v[224:227], 0\n"
    "buffer_load_dwordx4 a[72:75], v12, s[32:35], s45 offen sc1\n"
    "v_exp_f32 v122, v96\n"
    "v_exp_f32 v123, v97\n"
    "v_cvt_pk_bf16_f32 v119, v120, v121\n"
    "v_exp_f32 v124, v98\n"
    "v_exp_f32 v125, v99\n"
    "v_cvt_pk_bf16_f32 v112, v122, v123\n"
    "v_mfma_f32_32x32x16_bf16 v[160:175], v[28:31], v[116:119], v[160:175]\n"
    "buffer_load_dwordx4 a[76:79], v13, s[32:35], s45 offen sc1\n"
    "v_exp_f32 v120, v100\n"
    "v_exp_f32 v121, v101\n"
    "v_cvt_pk_bf16_f32 v113, v124, v125\n"
    "v_exp_f32 v122, v102\n"
    "v_exp_f32 v123, v103\n"
    "v_cvt_pk_bf16_f32 v114, v120, v121\n"
    "v_mfma_f32_32x32x16_bf16 v[80:95], v[36:39], v[228:231], v[80:95]\n"
    "buffer_load_dwordx4 v[76:79], v195, s[8:11], s84 offen offset:3072 sc1\n"
    "v_exp_f32 v124, v104\n"
    "v_exp_f32 v125, v105\n"
    "v_cvt_pk_bf16_f32 v115, v122, v123\n"
    "v_exp_f32 v120, v106\n"
    "v_exp_f32 v121, v107\n"
    "v_cvt_pk_bf16_f32 v116, v124, v125\n"
    "v_mfma_f32_32x32x16_bf16 v[176:191], v[24:27], v[112:115], v[176:191]\n"
    "ds_read_b64_tr_b16 a[240:241], v210 offset:20480\n"
    "v_exp_f32 v122, v108\n"
    "v_exp_f32 v123, v109\n"
    "v_cvt_pk_bf16_f32 v117, v120, v121\n"
    "v_exp_f32 v124, v110\n"
    "v_exp_f32 v125, v111\n"
    "v_cvt_pk_bf16_f32 v118, v122, v123\n"
    "v_mfma_f32_32x32x16_bf16 v[96:111], v[32:35], v[232:235], 0\n"
    "buffer_load_dwordx4 a[80:83], v10, s[32:35], s45 offen offset:1024 sc1\n"
    "v_exp_f32 v120, v80\n"
    "v_exp_f32 v121, v81\n"
    "v_cvt_pk_bf16_f32 v119, v124, v125\n"
    "v_exp_f32 v122, v82\n"
    "v_exp_f32 v123, v83\n"
    "v_cvt_pk_bf16_f32 v112, v120, v121\n"
    "v_mfma_f32_32x32x16_bf16 v[176:191], v[28:31], v[116:119], v[176:191]\n"
    "s_xor_b32 s84, s99, 49152\n"
    "v_exp_f32 v124, v84\n"
    "v_exp_f32 v125, v85\n"
    "v_cvt_pk_bf16_f32 v113, v122, v123\n"
    "v_exp_f32 v120, v86\n"
    "v_exp_f32 v121, v87\n"
    "v_cvt_pk_bf16_f32 v114, v124, v125\n"
    "v_mfma_f32_32x32x16_bf16 v[96:111], v[36:39], v[236:239], v[96:111]\n"
    "buffer_load_dwordx4 v[16:19], v195, s[8:11], s84 offen sc1\n"
    "v_exp_f32 v122, v88\n"
    "v_exp_f32 v123, v89\n"
    "v_cvt_pk_bf16_f32 v115, v120, v121\n"
    "v_exp_f32 v124, v90\n"
    "v_exp_f32 v125, v91\n"
    "v_cvt_pk_bf16_f32 v116, v122, v123\n"
    "v_mfma_f32_32x32x16_bf16 v[128:143], v[40:43], v[112:115], v[128:143]\n"
    "buffer_load_dwordx4 a[84:87], v11, s[32:35], s45 offen offset:1024 sc1\n"
    "v_exp_f32 v120, v92\n"
    "v_exp_f32 v121, v93\n"
    "v_cvt_pk_bf16_f32 v117, v124, v125\n"
    "v_exp_f32 v122, v94\n"
    "v_exp_f32 v123, v95\n"
    "v_cvt_pk_bf16_f32 v118, v120, v121\n"
    "v_mfma_f32_32x32x16_bf16 v[80:95], v[32:35], v[240:243], 0\n"
    "ds_read_b64_tr_b16 a[242:243], v210 offset:22528\n"
    "v_exp_f32 v124, v96\n"
    "v_exp_f32 v125, v97\n"
    "v_cvt_pk_bf16_f32 v119, v122, v123\n"
    "v_exp_f32 v120, v98\n"
    "v_exp_f32 v121, v99\n"
    "v_cvt_pk_bf16_f32 v112, v124, v125\n"
    "v_mfma_f32_32x32x16_bf16 v[128:143], v[44:47], v[116:119], v[128:143]\n"
    "buffer_load_dwordx4 a[88:91], v12, s[32:35], s45 offen offset:1024 sc1\n"
    "v_exp_f32 v122, v100\n"
    "v_exp_f32 v123, v101\n"
    "v_cvt_pk_bf16_f32 v113, v120, v121\n"
    "v_exp_f32 v124, v102\n"
    "v_exp_f32 v125, v103\n"
    "v_cvt_pk_bf16_f32 v114, v122, v123\n"
    "v_mfma_f32_32x32x16_bf16 v[80:95], v[36:39], v[244:247], v[80:95]\n"
    "buffer_load_dwordx4 v[20:23], v195, s[8:11], s84 offen offset:1024 sc1\n"
    "v_exp_f32 v120, v104\n"
    "v_exp_f32 v121, v105\n"
    "v_cvt_pk_bf16_f32 v115, v124, v125\n"
    "v_exp_f32 v122, v106\n"
    "v_exp_f32 v123, v107\n"
    "v_cvt_pk_bf16_f32 v116, v120, v121\n"
    "v_mfma_f32_32x32x16_bf16 v[144:159], v[40:43], v[112:115], v[144:159]\n"
    "buffer_load_dwordx4 a[92:95], v13, s[32:35], s45 offen offset:1024 sc1\n"
    "v_exp_f32 v124, v108\n"
    "v_exp_f32 v125, v109\n"
    "v_cvt_pk_bf16_f32 v117, v122, v123\n"
    "v_exp_f32 v120, v110\n"
    "v_exp_f32 v121, v111\n"
    "v_cvt_pk_bf16_f32 v118, v124, v125\n"
    "v_mfma_f32_32x32x16_bf16 v[96:111], v[32:35], v[248:251], 0\n"
    "ds_read_b64_tr_b16 a[244:245], v210 offset:24576\n"
    "v_exp_f32 v122, v80\n"
    "v_exp_f32 v123, v81\n"
    "v_cvt_pk_bf16_f32 v119, v120, v121\n"
    "v_exp_f32 v124, v82\n"
    "v_exp_f32 v125, v83\n"
    "v_cvt_pk_bf16_f32 v112, v122, v123\n"
    "v_mfma_f32_32x32x16_bf16 v[144:159], v[44:47], v[116:119], v[144:159]\n"
    "ds_read_b64_tr_b16 a[246:247], v210 offset:26624\n"
    "v_exp_f32 v120, v84\n"
    "v_exp_f32 v121, v85\n"
    "v_cvt_pk_bf16_f32 v113, v124, v125\n"
    "v_exp_f32 v122, v86\n"
    "v_exp_f32 v123, v87\n"
    "v_cvt_pk_bf16_f32 v114, v120, v121\n"
    "v_mfma_f32_32x32x16_bf16 v[96:111], v[36:39], v[252:255], v[96:111]\n"
    "buffer_load_dwordx4 v[24:27], v195, s[8:11], s84 offen offset:2048 sc1\n"
    "v_exp_f32 v124, v88\n"
    "v_exp_f32 v125, v89\n"
    "v_cvt_pk_bf16_f32 v115, v122, v123\n"
    "v_exp_f32 v120, v90\n"
    "v_exp_f32 v121, v91\n"
    "v_cvt_pk_bf16_f32 v116, v124, v125\n"
    "v_mfma_f32_32x32x16_bf16 v[160:175], v[40:43], v[112:115], v[160:175]\n"
    "s_waitcnt vmcnt(19)\n"
    "v_exp_f32 v122, v92\n"
    "v_exp_f32 v123, v93\n"
    "v_cvt_pk_bf16_f32 v117, v120, v121\n"
    "v_exp_f32 v124, v94\n"
    "v_exp_f32 v125, v95\n"
    "v_cvt_pk_bf16_f32 v118, v122, v123\n"
    "v_mfma_f32_32x32x16_bf16 v[80:95], v[48:51], v[224:227], 0\n"
    "buffer_load_dwordx4 a[96:99], v10, s[32:35], s46 offen sc1\n"
    "v_exp_f32 v120, v96\n"
    "v_exp_f32 v121, v97\n"
    "v_cvt_pk_bf16_f32 v119, v124, v125\n"
    "v_exp_f32 v122, v98\n"
    "v_exp_f32 v123, v99\n"
    "v_cvt_pk_bf16_f32 v112, v120, v121\n"
    "v_mfma_f32_32x32x16_bf16 v[160:175], v[44:47], v[116:119], v[160:175]\n"
    "buffer_load_dwordx4 a[100:103], v11, s[32:35], s46 offen sc1\n"
    "v_exp_f32 v124, v100\n"
    "v_exp_f32 v125, v101\n"
    "v_cvt_pk_bf16_f32 v113, v122, v123\n"
    "v_exp_f32 v120, v102\n"
    "v_exp_f32 v121, v103\n"
    "v_cvt_pk_bf16_f32 v114, v124, v125\n"
    "v_mfma_f32_32x32x16_bf16 v[80:95], v[52:55], v[228:231], v[80:95]\n"
    "buffer_load_dwordx4 v[28:31], v195, s[8:11], s84 offen offset:3072 sc1\n"
    "v_exp_f32 v122, v104\n"
    "v_exp_f32 v123, v105\n"
    "v_cvt_pk_bf16_f32 v115, v120, v121\n"
    "v_exp_f32 v124, v106\n"
    "v_exp_f32 v125, v107\n"
    "v_cvt_pk_bf16_f32 v116, v122, v123\n"
    "v_mfma_f32_32x32x16_bf16 v[176:191], v[40:43], v[112:115], v[176:191]\n"
    "ds_read_b64_tr_b16 a[248:249], v211 offset:20480\n"
    "v_exp_f32 v120, v108\n"
    "v_exp_f32 v121, v109\n"
    "v_cvt_pk_bf16_f32 v117, v124, v125\n"
    "v_exp_f32 v122, v110\n"
    "v_exp_f32 v123, v111\n"
    "v_cvt_pk_bf16_f32 v118, v120, v121\n"
    "v_mfma_f32_32x32x16_bf16 v[96:111], v[48:51], v[232:235], 0\n"
    "buffer_load_dwordx4 a[104:107], v12, s[32:35], s46 offen sc1\n"
    "v_exp_f32 v124, v80\n"
    "v_exp_f32 v125, v81\n"
    "v_cvt_pk_bf16_f32 v119, v122, v123\n"
    "v_exp_f32 v120, v82\n"
    "v_exp_f32 v121, v83\n"
    "v_cvt_pk_bf16_f32 v112, v124, v125\n"
    "v_mfma_f32_32x32x16_bf16 v[176:191], v[44:47], v[116:119], v[176:191]\n"
    "s_xor_b32 s84, s99, 53248\n"
    "v_exp_f32 v122, v84\n"
    "v_exp_f32 v123, v85\n"
    "v_cvt_pk_bf16_f32 v113, v120, v121\n"
    "v_exp_f32 v124, v86\n"
    "v_exp_f32 v125, v87\n"
    "v_cvt_pk_bf16_f32 v114, v122, v123\n"
    "v_mfma_f32_32x32x16_bf16 v[96:111], v[52:55], v[236:239], v[96:111]\n"
    "buffer_load_dwordx4 v[32:35], v195, s[8:11], s84 offen sc1\n"
    "v_exp_f32 v120, v88\n"
    "v_exp_f32 v121, v89\n"
    "v_cvt_pk_bf16_f32 v115, v124, v125\n"
    "v_exp_f32 v122, v90\n"
    "v_exp_f32 v123, v91\n"
    "v_cvt_pk_bf16_f32 v116, v120, v121\n"
    "v_mfma_f32_32x32x16_bf16 v[128:143], v[56:59], v[112:115], v[128:143]\n"
    "buffer_load_dwordx4 a[108:111], v13, s[32:35], s46 offen sc1\n"
    "v_exp_f32 v124, v92\n"
    "v_exp_f32 v125, v93\n"
    "v_cvt_pk_bf16_f32 v117, v122, v123\n"
    "v_exp_f32 v120, v94\n"
    "v_exp_f32 v121, v95\n"
    "v_cvt_pk_bf16_f32 v118, v124, v125\n"
    "v_mfma_f32_32x32x16_bf16 v[80:95], v[48:51], v[240:243], 0\n"
    "buffer_load_dwordx4 a[112:115], v10, s[32:35], s46 offen offset:1024 sc1\n"
    "v_exp_f32 v122, v96\n"
    "v_exp_f32 v123, v97\n"
    "v_cvt_pk_bf16_f32 v119, v120, v121\n"
    "v_exp_f32 v124, v98\n"
    "v_exp_f32 v125, v99\n"
    "v_cvt_pk_bf16_f32 v112, v122, v123\n"
    "v_mfma_f32_32x32x16_bf16 v[128:143], v[60:63], v[116:119], v[128:143]\n"
    "ds_read_b64_tr_b16 a[250:251], v211 offset:22528\n"
    "v_exp_f32 v120, v100\n"
    "v_exp_f32 v121, v101\n"
    "v_cvt_pk_bf16_f32 v113, v124, v125\n"
    "v_exp_f32 v122, v102\n"
    "v_exp_f32 v123, v103\n"
    "v_cvt_pk_bf16_f32 v114, v120, v121\n"
    "v_mfma_f32_32x32x16_bf16 v[80:95], v[52:55], v[244:247], v[80:95]\n"
    "buffer_load_dwordx4 v[36:39], v195, s[8:11], s84 offen offset:1024 sc1\n"
    "v_exp_f32 v124, v104\n"
    "v_exp_f32 v125, v105\n"
    "v_cvt_pk_bf16_f32 v115, v122, v123\n"
    "v_exp_f32 v120, v106\n"
    "v_exp_f32 v121, v107\n"
    "v_cvt_pk_bf16_f32 v116, v124, v125\n"
    "v_mfma_f32_32x32x16_bf16 v[144:159], v[56:59], v[112:115], v[144:159]\n"
    "buffer_load_dwordx4 a[116:119], v11, s[32:35], s46 offen offset:1024 sc1\n"
    "v_exp_f32 v122, v108\n"
    "v_exp_f32 v123, v109\n"
    "v_cvt_pk_bf16_f32 v117, v120, v121\n"
    "v_exp_f32 v124, v110\n"
    "v_exp_f32 v125, v111\n"
    "v_cvt_pk_bf16_f32 v118, v122, v123\n"
    "v_mfma_f32_32x32x16_bf16 v[96:111], v[48:51], v[248:251], 0\n"
    "ds_read_b64_tr_b16 a[252:253], v211 offset:24576\n"
    "v_exp_f32 v120, v80\n"
    "v_exp_f32 v121, v81\n"
    "v_cvt_pk_bf16_f32 v119, v124, v125\n"
    "v_exp_f32 v122, v82\n"
    "v_exp_f32 v123, v83\n"
    "v_cvt_pk_bf16_f32 v112, v120, v121\n"
    "v_mfma_f32_32x32x16_bf16 v[144:159], v[60:63], v[116:119], v[144:159]\n"
    "buffer_load_dwordx4 a[120:123], v12, s[32:35], s46 offen offset:1024 sc1\n"
    "v_exp_f32 v124, v84\n"
    "v_exp_f32 v125, v85\n"
    "v_cvt_pk_bf16_f32 v113, v122, v123\n"
    "v_exp_f32 v120, v86\n"
    "v_exp_f32 v121, v87\n"
    "v_cvt_pk_bf16_f32 v114, v124, v125\n"
    "v_mfma_f32_32x32x16_bf16 v[96:111], v[52:55], v[252:255], v[96:111]\n"
    "buffer_load_dwordx4 v[40:43], v195, s[8:11], s84 offen offset:2048 sc1\n"
    "v_exp_f32 v122, v88\n"
    "v_exp_f32 v123, v89\n"
    "v_cvt_pk_bf16_f32 v115, v120, v121\n"
    "v_exp_f32 v124, v90\n"
    "v_exp_f32 v125, v91\n"
    "v_cvt_pk_bf16_f32 v116, v122, v123\n"
    "v_mfma_f32_32x32x16_bf16 v[160:175], v[56:59], v[112:115], v[160:175]\n"
    "s_waitcnt vmcnt(18)\n"
    "v_exp_f32 v120, v92\n"
    "v_exp_f32 v121, v93\n"
    "v_cvt_pk_bf16_f32 v117, v124, v125\n"
    "v_exp_f32 v122, v94\n"
    "v_exp_f32 v123, v95\n"
    "v_cvt_pk_bf16_f32 v118, v120, v121\n"
    "v_mfma_f32_32x32x16_bf16 v[80:95], v[64:67], v[224:227], 0\n"
    "buffer_load_dwordx4 a[124:127], v13, s[32:35], s46 offen offset:1024 sc1\n"
    "v_exp_f32 v124, v96\n"
    "v_exp_f32 v125, v97\n"
    "v_cvt_pk_bf16_f32 v119, v122, v123\n"
    "v_exp_f32 v120, v98\n"
    "v_exp_f32 v121, v99\n"
    "v_cvt_pk_bf16_f32 v112, v124, v125\n"
    "v_mfma_f32_32x32x16_bf16 v[160:175], v[60:63], v[116:119], v[160:175]\n"
    "ds_read_b64_tr_b16 a[254:255], v211 offset:26624\n"
    "v_exp_f32 v122, v100\n"
    "v_exp_f32 v123, v101\n"
    "v_cvt_pk_bf16_f32 v113, v120, v121\n"
    "v_exp_f32 v124, v102\n"
    "v_exp_f32 v125, v103\n"
    "v_cvt_pk_bf16_f32 v114, v122, v123\n"
    "v_mfma_f32_32x32x16_bf16 v[80:95], v[68:71], v[228:231], v[80:95]\n"
    "buffer_load_dwordx4 v[44:47], v195, s[8:11], s84 offen offset:3072 sc1\n"
    "v_exp_f32 v120, v104\n"
    "v_exp_f32 v121, v105\n"
    "v_cvt_pk_bf16_f32 v115, v124, v125\n"
    "v_exp_f32 v122, v106\n"
    "v_exp_f32 v123, v107\n"
    "v_cvt_pk_bf16_f32 v116, v120, v121\n"
    "v_mfma_f32_32x32x16_bf16 v[176:191], v[56:59], v[112:115], v[176:191]\n"
    "buffer_load_dwordx4 a[128:131], v10, s[32:35], s47 offen sc1\n"
    "v_exp_f32 v124, v108\n"
    "v_exp_f32 v125, v109\n"
    "v_cvt_pk_bf16_f32 v117, v122, v123\n"
    "v_exp_f32 v120, v110\n"
    "v_exp_f32 v121, v111\n"
    "v_cvt_pk_bf16_f32 v118, v124, v125\n"
    "v_mfma_f32_32x32x16_bf16 v[96:111], v[64:67], v[232:235], 0\n"
    "s_waitcnt lgkmcnt(0)\n"
    "v_exp_f32 v122, v80\n"
    "v_exp_f32 v123, v81\n"
    "v_cvt_pk_bf16_f32 v119, v120, v121\n"
    "v_exp_f32 v124, v82\n"
    "v_exp_f32 v125, v83\n"
    "v_cvt_pk_bf16_f32 v112, v122, v123\n"
    "v_mfma_f32_32x32x16_bf16 v[176:191], v[60:63], v[116:119], v[176:191]\n"
    "s_xor_b32 s84, s99, 57344\n"
    "v_exp_f32 v120, v84\n"
    "v_exp_f32 v121, v85\n"
    "v_cvt_pk_bf16_f32 v113, v124, v125\n"
    "v_exp_f32 v122, v86\n"
    "v_exp_f32 v123, v87\n"
    "v_cvt_pk_bf16_f32 v114, v120, v121\n"
    "v_mfma_f32_32x32x16_bf16 v[96:111], v[68:71], v[236:239], v[96:111]\n"
    "buffer_load_dwordx4 v[48:51], v195, s[8:11], s84 offen sc1\n"
    "v_exp_f32 v124, v88\n"
    "v_exp_f32 v125, v89\n"
    "v_cvt_pk_bf16_f32 v115, v122, v123\n"
    "v_exp_f32 v120, v90\n"
    "v_exp_f32 v121, v91\n"
    "v_cvt_pk_bf16_f32 v116, v124, v125\n"
    "v_mfma_f32_32x32x16_bf16 v[128:143], v[72:75], v[112:115], v[128:143]\n"
    "buffer_load_dwordx4 a[132:135], v11, s[32:35], s47 offen sc1\n"
    "v_exp_f32 v122, v92\n"
    "v_exp_f32 v123, v93\n"
    "v_cvt_pk_bf16_f32 v117, v120, v121\n"
    "v_exp_f32 v124, v94\n"
    "v_exp_f32 v125, v95\n"
    "v_cvt_pk_bf16_f32 v118, v122, v123\n"
    "v_mfma_f32_32x32x16_bf16 v[80:95], v[64:67], v[240:243], 0\n"
    "buffer_load_dwordx4 a[136:139], v12, s[32:35], s47 offen sc1\n"
    "v_exp_f32 v120, v96\n"
    "v_exp_f32 v121, v97\n"
    "v_cvt_pk_bf16_f32 v119, v124, v125\n"
    "v_exp_f32 v122, v98\n"
    "v_exp_f32 v123, v99\n"
    "v_cvt_pk_bf16_f32 v112, v120, v121\n"
    "v_mfma_f32_32x32x16_bf16 v[128:143], v[76:79], v[116:119], v[128:143]\n"
    "s_add_u32 s39, s39, 28672\n"
    "v_exp_f32 v124, v100\n"
    "v_exp_f32 v125, v101\n"
    "v_cvt_pk_bf16_f32 v113, v122, v123\n"
    "v_exp_f32 v120, v102\n"
    "v_exp_f32 v121, v103\n"
    "v_cvt_pk_bf16_f32 v114, v124, v125\n"
    "v_mfma_f32_32x32x16_bf16 v[80:95], v[68:71], v[244:247], v[80:95]\n"
    "buffer_load_dwordx4 v[52:55], v195, s[8:11], s84 offen offset:1024 sc1\n"
    "v_exp_f32 v122, v104\n"
    "v_exp_f32 v123, v105\n"
    "v_cvt_pk_bf16_f32 v115, v120, v121\n"
    "v_exp_f32 v124, v106\n"
    "v_exp_f32 v125, v107\n"
    "v_cvt_pk_bf16_f32 v116, v122, v123\n"
    "v_mfma_f32_32x32x16_bf16 v[144:159], v[72:75], v[112:115], v[144:159]\n"
    "buffer_load_dwordx4 a[140:143], v13, s[32:35], s47 offen sc1\n"
    "v_exp_f32 v120, v108\n"
    "v_exp_f32 v121, v109\n"
    "v_cvt_pk_bf16_f32 v117, v124, v125\n"
    "v_exp_f32 v122, v110\n"
    "v_exp_f32 v123, v111\n"
    "v_cvt_pk_bf16_f32 v118, v120, v121\n"
    "v_mfma_f32_32x32x16_bf16 v[96:111], v[64:67], v[248:251], 0\n"
    "buffer_load_dwordx4 a[144:147], v10, s[32:35], s47 offen offset:1024 sc1\n"
    "v_exp_f32 v124, v80\n"
    "v_exp_f32 v125, v81\n"
    "v_cvt_pk_bf16_f32 v119, v122, v123\n"
    "v_exp_f32 v120, v82\n"
    "v_exp_f32 v121, v83\n"
    "v_cvt_pk_bf16_f32 v112, v124, v125\n"
    "v_mfma_f32_32x32x16_bf16 v[144:159], v[76:79], v[116:119], v[144:159]\n"
    "s_add_u32 m0, s78, 20480\n"
    "v_exp_f32 v122, v84\n"
    "v_exp_f32 v123, v85\n"
    "v_cvt_pk_bf16_f32 v113, v120, v121\n"
    "v_exp_f32 v124, v86\n"
    "v_exp_f32 v125, v87\n"
    "v_cvt_pk_bf16_f32 v114, v122, v123\n"
    "v_mfma_f32_32x32x16_bf16 v[96:111], v[68:71], v[252:255], v[96:111]\n"
    "buffer_load_dwordx4 v[56:59], v195, s[8:11], s84 offen offset:2048 sc1\n"
    "v_exp_f32 v120, v88\n"
    "v_exp_f32 v121, v89\n"
    "v_cvt_pk_bf16_f32 v115, v124, v125\n"
    "v_exp_f32 v122, v90\n"
    "v_exp_f32 v123, v91\n"
    ".Lpage_6_%=:\n"
    "s_cbranch_execz .Lpage_0_%=\n"
    "v_cvt_pk_bf16_f32 v116, v120, v121\n"
    "v_mfma_f32_32x32x16_bf16 v[160:175], v[72:75], v[112:115], v[160:175]\n"
    "s_waitcnt vmcnt(18)\n"
    "v_exp_f32 v124, v92\n"
    "v_exp_f32 v125, v93\n"
    "v_cvt_pk_bf16_f32 v117, v122, v123\n"
    "v_exp_f32 v120, v94\n"
    "v_exp_f32 v121, v95\n"
    "v_cvt_pk_bf16_f32 v118, v124, v125\n"
    "v_mfma_f32_32x32x16_bf16 v[80:95], v[16:19], v[224:227], 0\n"
    "buffer_load_dwordx4 a[148:151], v11, s[32:35], s47 offen offset:1024 sc1\n"
    "v_exp_f32 v122, v96\n"
    "v_exp_f32 v123, v97\n"
    "v_cvt_pk_bf16_f32 v119, v120, v121\n"
    "v_exp_f32 v124, v98\n"
    "v_exp_f32 v125, v99\n"
    "v_cvt_pk_bf16_f32 v112, v122, v123\n"
    "v_mfma_f32_32x32x16_bf16 v[160:175], v[76:79], v[116:119], v[160:175]\n"
    "v_exp_f32 v120, v100\n"
    "v_exp_f32 v121, v101\n"
    "v_cvt_pk_bf16_f32 v113, v124, v125\n"
    "v_exp_f32 v122, v102\n"
    "v_exp_f32 v123, v103\n"
    "v_cvt_pk_bf16_f32 v114, v120, v121\n"
    "v_mfma_f32_32x32x16_bf16 v[80:95], v[20:23], v[228:231], v[80:95]\n"
    "buffer_load_dwordx4 v[60:63], v195, s[8:11], s84 offen offset:3072 sc1\n"
    "v_exp_f32 v124, v104\n"
    "v_exp_f32 v125, v105\n"
    "v_cvt_pk_bf16_f32 v115, v122, v123\n"
    "v_exp_f32 v120, v106\n"
    "v_exp_f32 v121, v107\n"
    "v_cvt_pk_bf16_f32 v116, v124, v125\n"
    "v_mfma_f32_32x32x16_bf16 v[176:191], v[72:75], v[112:115], v[176:191]\n"
    "buffer_load_dwordx4 a[152:155], v12, s[32:35], s47 offen offset:1024 sc1\n"
    "v_exp_f32 v122, v108\n"
    "v_exp_f32 v123, v109\n"
    "v_cvt_pk_bf16_f32 v117, v120, v121\n"
    "v_exp_f32 v124, v110\n"
    "v_exp_f32 v125, v111\n"
    "v_cvt_pk_bf16_f32 v118, v122, v123\n"
    "v_mfma_f32_32x32x16_bf16 v[96:111], v[16:19], v[232:235], 0\n"
    "buffer_load_dwordx4 a[156:159], v13, s[32:35], s47 offen offset:1024 sc1\n"
    "v_exp_f32 v120, v80\n"
    "v_exp_f32 v121, v81\n"
    "v_cvt_pk_bf16_f32 v119, v124, v125\n"
    "v_exp_f32 v122, v82\n"
    "v_exp_f32 v123, v83\n"
    "v_cvt_pk_bf16_f32 v112, v120, v121\n"
    "v_mfma_f32_32x32x16_bf16 v[176:191], v[76:79], v[116:119], v[176:191]\n"
    "s_xor_b32 s84, s99, 61440\n"
    "v_exp_f32 v124, v84\n"
    "v_exp_f32 v125, v85\n"
    "v_cvt_pk_bf16_f32 v113, v122, v123\n"
    "v_exp_f32 v120, v86\n"
    "v_exp_f32 v121, v87\n"
    "v_cvt_pk_bf16_f32 v114, v124, v125\n"
    "v_mfma_f32_32x32x16_bf16 v[96:111], v[20:23], v[236:239], v[96:111]\n"
    "buffer_load_dwordx4 v[64:67], v195, s[8:11], s84 offen sc1\n"
    "v_exp_f32 v122, v88\n"
    "v_exp_f32 v123, v89\n"
    "v_cvt_pk_bf16_f32 v115, v120, v121\n"
    "v_exp_f32 v124, v90\n"
    "v_exp_f32 v125, v91\n"
    "v_cvt_pk_bf16_f32 v116, v122, v123\n"
    "v_mfma_f32_32x32x16_bf16 v[128:143], v[24:27], v[112:115], v[128:143]\n"
    "buffer_load_dwordx4 a[160:163], v10, s[32:35], s0 offen sc1\n"
    "v_exp_f32 v120, v92\n"
    "v_exp_f32 v121, v93\n"
    "v_cvt_pk_bf16_f32 v117, v124, v125\n"
    "v_exp_f32 v122, v94\n"
    "v_exp_f32 v123, v95\n"
    "v_cvt_pk_bf16_f32 v118, v120, v121\n"
    "v_mfma_f32_32x32x16_bf16 v[80:95], v[16:19], v[240:243], 0\n"
    "buffer_load_dwordx4 v[68:71], v195, s[8:11], s84 offen offset:1024 sc1\n"
    "v_exp_f32 v124, v96\n"
    "v_exp_f32 v125, v97\n"
    "v_cvt_pk_bf16_f32 v119, v122, v123\n"
    "v_exp_f32 v120, v98\n"
    "v_exp_f32 v121, v99\n"
    "v_cvt_pk_bf16_f32 v112, v124, v125\n"
    "v_mfma_f32_32x32x16_bf16 v[128:143], v[28:31], v[116:119], v[128:143]\n"
    "v_exp_f32 v122, v100\n"
    "v_exp_f32 v123, v101\n"
    "v_cvt_pk_bf16_f32 v113, v120, v121\n"
    "v_exp_f32 v124, v102\n"
    "v_exp_f32 v125, v103\n"
    "v_cvt_pk_bf16_f32 v114, v122, v123\n"
    "v_mfma_f32_32x32x16_bf16 v[80:95], v[20:23], v[244:247], v[80:95]\n"
    "buffer_load_dwordx4 v[72:75], v195, s[8:11], s84 offen offset:2048 sc1\n"
    "v_exp_f32 v120, v104\n"
    "v_exp_f32 v121, v105\n"
    "v_cvt_pk_bf16_f32 v115, v124, v125\n"
    "v_exp_f32 v122, v106\n"
    "v_exp_f32 v123, v107\n"
    "v_cvt_pk_bf16_f32 v116, v120, v121\n"
    "v_mfma_f32_32x32x16_bf16 v[144:159], v[24:27], v[112:115], v[144:159]\n"
    "s_waitcnt vmcnt(16)\n"
    "v_exp_f32 v124, v108\n"
    "v_exp_f32 v125, v109\n"
    "v_cvt_pk_bf16_f32 v117, v122, v123\n"
    "v_exp_f32 v120, v110\n"
    "v_exp_f32 v121, v111\n"
    "v_cvt_pk_bf16_f32 v118, v124, v125\n"
    "v_mfma_f32_32x32x16_bf16 v[96:111], v[32:35], v[224:227], 0\n"
    "buffer_load_dwordx4 v[76:79], v195, s[8:11], s84 offen offset:3072 sc1\n"
    "v_exp_f32 v122, v80\n"
    "v_exp_f32 v123, v81\n"
    "v_cvt_pk_bf16_f32 v119, v120, v121\n"
    "v_exp_f32 v124, v82\n"
    "v_exp_f32 v125, v83\n"
    "v_cvt_pk_bf16_f32 v112, v122, v123\n"
    "v_mfma_f32_32x32x16_bf16 v[144:159], v[28:31], v[116:119], v[144:159]\n"
    "buffer_load_dwordx4 a[164:167], v11, s[32:35], s0 offen sc1\n"
    "v_exp_f32 v120, v84\n"
    "v_exp_f32 v121, v85\n"
    "v_cvt_pk_bf16_f32 v113, v124, v125\n"
    "v_exp_f32 v122, v86\n"
    "v_exp_f32 v123, v87\n"
    "v_cvt_pk_bf16_f32 v114, v120, v121\n"
    "v_mfma_f32_32x32x16_bf16 v[96:111], v[36:39], v[228:231], v[96:111]\n"
    "buffer_load_dwordx4 a[168:171], v12, s[32:35], s0 offen sc1\n"
    "v_exp_f32 v124, v88\n"
    "v_exp_f32 v125, v89\n"
    "v_cvt_pk_bf16_f32 v115, v122, v123\n"
    "v_exp_f32 v120, v90\n"
    "v_exp_f32 v121, v91\n"
    "v_cvt_pk_bf16_f32 v116, v124, v125\n"
    "v_mfma_f32_32x32x16_bf16 v[160:175], v[24:27], v[112:115], v[160:175]\n"
    "v_exp_f32 v122, v92\n"
    "v_exp_f32 v123, v93\n"
    "v_cvt_pk_bf16_f32 v117, v120, v121\n"
    "v_exp_f32 v124, v94\n"
    "v_exp_f32 v125, v95\n"
    "v_cvt_pk_bf16_f32 v118, v122, v123\n"
    "v_mfma_f32_32x32x16_bf16 v[80:95], v[32:35], v[232:235], 0\n"
    "buffer_load_dwordx4 a[172:175], v13, s[32:35], s0 offen sc1\n"
    "v_exp_f32 v120, v96\n"
    "v_exp_f32 v121, v97\n"
    "v_cvt_pk_bf16_f32 v119, v124, v125\n"
    "v_exp_f32 v122, v98\n"
    "v_exp_f32 v123, v99\n"
    "v_cvt_pk_bf16_f32 v112, v120, v121\n"
    "v_mfma_f32_32x32x16_bf16 v[160:175], v[28:31], v[116:119], v[160:175]\n"
    "buffer_load_dwordx4 a[176:179], v10, s[32:35], s0 offen offset:1024 sc1\n"
    "v_exp_f32 v124, v100\n"
    "v_exp_f32 v125, v101\n"
    "v_cvt_pk_bf16_f32 v113, v122, v123\n"
    "v_exp_f32 v120, v102\n"
    "v_exp_f32 v121, v103\n"
    "v_cvt_pk_bf16_f32 v114, v124, v125\n"
    "v_mfma_f32_32x32x16_bf16 v[80:95], v[36:39], v[236:239], v[80:95]\n"
    "v_exp_f32 v122, v104\n"
    "v_exp_f32 v123, v105\n"
    "v_cvt_pk_bf16_f32 v115, v120, v121\n"
    "v_exp_f32 v124, v106\n"
    "v_exp_f32 v125, v107\n"
    "v_cvt_pk_bf16_f32 v116, v122, v123\n"
    "v_mfma_f32_32x32x16_bf16 v[128:143], v[40:43], v[112:115], v[128:143]\n"
    "buffer_load_dwordx4 a[180:183], v11, s[32:35], s0 offen offset:1024 sc1\n"
    "v_exp_f32 v120, v108\n"
    "v_exp_f32 v121, v109\n"
    "v_cvt_pk_bf16_f32 v117, v124, v125\n"
    "v_exp_f32 v122, v110\n"
    "v_exp_f32 v123, v111\n"
    "v_cvt_pk_bf16_f32 v118, v120, v121\n"
    "v_mfma_f32_32x32x16_bf16 v[96:111], v[32:35], v[240:243], 0\n"
    "buffer_load_dwordx4 v204, s[20:23], s39 offen lds\n"
    "v_exp_f32 v124, v80\n"
    "v_exp_f32 v125, v81\n"
    "v_cvt_pk_bf16_f32 v119, v122, v123\n"
    "v_exp_f32 v120, v82\n"
    "v_exp_f32 v121, v83\n"
    "v_cvt_pk_bf16_f32 v112, v124, v125\n"
    "v_mfma_f32_32x32x16_bf16 v[128:143], v[44:47], v[116:119], v[128:143]\n"
    "v_exp_f32 v122, v84\n"
    "v_exp_f32 v123, v85\n"
    "v_cvt_pk_bf16_f32 v113, v120, v121\n"
    "v_exp_f32 v124, v86\n"
    "v_exp_f32 v125, v87\n"
    "v_cvt_pk_bf16_f32 v114, v122, v123\n"
    "v_mfma_f32_32x32x16_bf16 v[96:111], v[36:39], v[244:247], v[96:111]\n"
    "buffer_load_dwordx4 a[184:187], v12, s[32:35], s0 offen offset:1024 sc1\n"
    "v_exp_f32 v120, v88\n"
    "v_exp_f32 v121, v89\n"
    "v_cvt_pk_bf16_f32 v115, v124, v125\n"
    "v_exp_f32 v122, v90\n"
    "v_exp_f32 v123, v91\n"
    "v_cvt_pk_bf16_f32 v116, v120, v121\n"
    "v_mfma_f32_32x32x16_bf16 v[144:159], v[40:43], v[112:115], v[144:159]\n"
    "s_waitcnt vmcnt(14)\n"
    "v_exp_f32 v124, v92\n"
    "v_exp_f32 v125, v93\n"
    "v_cvt_pk_bf16_f32 v117, v122, v123\n"
    "v_exp_f32 v120, v94\n"
    "v_exp_f32 v121, v95\n"
    "v_cvt_pk_bf16_f32 v118, v124, v125\n"
    "v_mfma_f32_32x32x16_bf16 v[80:95], v[48:51], v[224:227], 0\n"
    "buffer_load_dwordx4 v205, s[20:23], s39 offen offset:1024 lds\n"
    "v_exp_f32 v122, v96\n"
    "v_exp_f32 v123, v97\n"
    "v_cvt_pk_bf16_f32 v119, v120, v121\n"
    "v_exp_f32 v124, v98\n"
    "v_exp_f32 v125, v99\n"
    "v_cvt_pk_bf16_f32 v112, v122, v123\n"
    "v_mfma_f32_32x32x16_bf16 v[144:159], v[44:47], v[116:119], v[144:159]\n"
    "buffer_load_dwordx4 a[188:191], v13, s[32:35], s0 offen offset:1024 sc1\n"
    "v_exp_f32 v120, v100\n"
    "v_exp_f32 v121, v101\n"
    "v_cvt_pk_bf16_f32 v113, v124, v125\n"
    "v_exp_f32 v122, v102\n"
    "v_exp_f32 v123, v103\n"
    "v_cvt_pk_bf16_f32 v114, v120, v121\n"
    "v_mfma_f32_32x32x16_bf16 v[80:95], v[52:55], v[228:231], v[80:95]\n"
    "buffer_load_dwordx4 v206, s[20:23], s39 offen offset:2048 lds\n"
    "v_exp_f32 v124, v104\n"
    "v_exp_f32 v125, v105\n"
    "v_cvt_pk_bf16_f32 v115, v122, v123\n"
    "v_exp_f32 v120, v106\n"
    "v_exp_f32 v121, v107\n"
    "v_cvt_pk_bf16_f32 v116, v124, v125\n"
    "v_mfma_f32_32x32x16_bf16 v[160:175], v[40:43], v[112:115], v[160:175]\n"
    "s_waitcnt vmcnt(10)\n"
    "v_exp_f32 v122, v108\n"
    "v_exp_f32 v123, v109\n"
    "v_cvt_pk_bf16_f32 v117, v120, v121\n"
    "v_exp_f32 v124, v110\n"
    "v_exp_f32 v125, v111\n"
    "v_cvt_pk_bf16_f32 v118, v122, v123\n"
    "v_mfma_f32_32x32x16_bf16 v[96:111], v[64:67], v[224:227], 0\n"
    "buffer_load_dwordx4 a[192:195], v10, s[32:35], s1 offen sc1\n"
    "v_exp_f32 v120, v80\n"
    "v_exp_f32 v121, v81\n"
    "v_cvt_pk_bf16_f32 v119, v124, v125\n"
    "v_exp_f32 v122, v82\n"
    "v_exp_f32 v123, v83\n"
    "v_cvt_pk_bf16_f32 v112, v120, v121\n"
    "v_mfma_f32_32x32x16_bf16 v[160:175], v[44:47], v[116:119], v[160:175]\n"
    "buffer_load_dwordx4 v207, s[20:23], s39 offen offset:3072 lds\n"
    "v_exp_f32 v124, v84\n"
    "v_exp_f32 v125, v85\n"
    "v_cvt_pk_bf16_f32 v113, v122, v123\n"
    "v_exp_f32 v120, v86\n"
    "v_exp_f32 v121, v87\n"
    "v_cvt_pk_bf16_f32 v114, v124, v125\n"
    "v_mfma_f32_32x32x16_bf16 v[96:111], v[68:71], v[228:231], v[96:111]\n"
    "buffer_load_dwordx4 a[196:199], v11, s[32:35], s1 offen sc1\n"
    "v_exp_f32 v122, v88\n"
    "v_exp_f32 v123, v89\n"
    "v_cvt_pk_bf16_f32 v115, v120, v121\n"
    "v_exp_f32 v124, v90\n"
    "v_exp_f32 v125, v91\n"
    "v_cvt_pk_bf16_f32 v116, v122, v123\n"
    "v_mfma_f32_32x32x16_bf16 v[128:143], v[56:59], v[112:115], v[128:143]\n"
    "s_add_u32 m0, s78, 24576\n"
    "v_exp_f32 v120, v92\n"
    "v_exp_f32 v121, v93\n"
    "v_cvt_pk_bf16_f32 v117, v124, v125\n"
    "v_exp_f32 v122, v94\n"
    "v_exp_f32 v123, v95\n"
    "v_cvt_pk_bf16_f32 v118, v120, v121\n"
    "v_mfma_f32_32x32x16_bf16 v[80:95], v[48:51], v[232:235], 0\n"
    "buffer_load_dwordx4 a[200:203], v12, s[32:35], s1 offen sc1\n"
    "v_exp_f32 v124, v96\n"
    "v_exp_f32 v125, v97\n"
    "v_cvt_pk_bf16_f32 v119, v122, v123\n"
    "v_exp_f32 v120, v98\n"
    "v_exp_f32 v121, v99\n"
    "v_cvt_pk_bf16_f32 v112, v124, v125\n"
    "v_mfma_f32_32x32x16_bf16 v[128:143], v[60:63], v[116:119], v[128:143]\n"
    "buffer_load_dwordx4 v204, s[20:23], s40 offen lds\n"
    "v_exp_f32 v122, v100\n"
    "v_exp_f32 v123, v101\n"
    "v_cvt_pk_bf16_f32 v113, v120, v121\n"
    "v_exp_f32 v124, v102\n"
    "v_exp_f32 v125, v103\n"
    "v_cvt_pk_bf16_f32 v114, v122, v123\n"
    "v_mfma_f32_32x32x16_bf16 v[80:95], v[52:55], v[236:239], v[80:95]\n"
    "v_exp_f32 v120, v104\n"
    "v_exp_f32 v121, v105\n"
    "v_cvt_pk_bf16_f32 v115, v124, v125\n"
    "v_exp_f32 v122, v106\n"
    "v_exp_f32 v123, v107\n"
    "v_cvt_pk_bf16_f32 v116, v120, v121\n"
    "v_mfma_f32_32x32x16_bf16 v[128:143], v[72:75], v[112:115], v[128:143]\n"
    "buffer_load_dwordx4 a[204:207], v13, s[32:35], s1 offen sc1\n"
    "v_exp_f32 v124, v108\n"
    "v_exp_f32 v125, v109\n"
    "v_cvt_pk_bf16_f32 v117, v122, v123\n"
    "v_exp_f32 v120, v110\n"
    "v_exp_f32 v121, v111\n"
    "v_cvt_pk_bf16_f32 v118, v124, v125\n"
    "v_mfma_f32_32x32x16_bf16 v[96:111], v[64:67], v[232:235], 0\n"
    "buffer_load_dwordx4 v205, s[20:23], s40 offen offset:1024 lds\n"
    "v_exp_f32 v122, v80\n"
    "v_exp_f32 v123, v81\n"
    "v_cvt_pk_bf16_f32 v119, v120, v121\n"
    "v_exp_f32 v124, v82\n"
    "v_exp_f32 v125, v83\n"
    "v_cvt_pk_bf16_f32 v112, v122, v123\n"
    "v_mfma_f32_32x32x16_bf16 v[128:143], v[76:79], v[116:119], v[128:143]\n"
    "buffer_load_dwordx4 a[208:211], v10, s[32:35], s1 offen offset:1024 sc1\n"
    "v_exp_f32 v120, v84\n"
    "v_exp_f32 v121, v85\n"
    "v_cvt_pk_bf16_f32 v113, v124, v125\n"
    "v_exp_f32 v122, v86\n"
    "v_exp_f32 v123, v87\n"
    "v_cvt_pk_bf16_f32 v114, v120, v121\n"
    "v_mfma_f32_32x32x16_bf16 v[96:111], v[68:71], v[236:239], v[96:111]\n"
    "v_exp_f32 v124, v88\n"
    "v_exp_f32 v125, v89\n"
    "v_cvt_pk_bf16_f32 v115, v122, v123\n"
    "v_exp_f32 v120, v90\n"
    "v_exp_f32 v121, v91\n"
    "v_cvt_pk_bf16_f32 v116, v124, v125\n"
    "v_mfma_f32_32x32x16_bf16 v[144:159], v[56:59], v[112:115], v[144:159]\n"
    "ds_write_b128 v212, v[128:131] offset:8192\n"
    "v_exp_f32 v122, v92\n"
    "v_exp_f32 v123, v93\n"
    "v_cvt_pk_bf16_f32 v117, v120, v121\n"
    "v_exp_f32 v124, v94\n"
    "v_exp_f32 v125, v95\n"
    "v_cvt_pk_bf16_f32 v118, v122, v123\n"
    "v_mfma_f32_32x32x16_bf16 v[80:95], v[48:51], v[240:243], 0\n"
    "buffer_load_dwordx4 a[212:215], v11, s[32:35], s1 offen offset:1024 sc1\n"
    "v_exp_f32 v120, v96\n"
    "v_exp_f32 v121, v97\n"
    "v_cvt_pk_bf16_f32 v119, v124, v125\n"
    "v_exp_f32 v122, v98\n"
    "v_exp_f32 v123, v99\n"
    "v_cvt_pk_bf16_f32 v112, v120, v121\n"
    "v_mfma_f32_32x32x16_bf16 v[144:159], v[60:63], v[116:119], v[144:159]\n"
    "ds_write_b128 v212, v[132:135] offset:9216\n"
    "v_exp_f32 v124, v100\n"
    "v_exp_f32 v125, v101\n"
    "v_cvt_pk_bf16_f32 v113, v122, v123\n"
    "v_exp_f32 v120, v102\n"
    "v_exp_f32 v121, v103\n"
    "v_cvt_pk_bf16_f32 v114, v124, v125\n"
    "v_mfma_f32_32x32x16_bf16 v[80:95], v[52:55], v[244:247], v[80:95]\n"
    "buffer_load_dwordx4 a[216:219], v12, s[32:35], s1 offen offset:1024 sc1\n"
    "v_exp_f32 v122, v104\n"
    "v_exp_f32 v123, v105\n"
    "v_cvt_pk_bf16_f32 v115, v120, v121\n"
    "v_exp_f32 v124, v106\n"
    "v_exp_f32 v125, v107\n"
    "v_cvt_pk_bf16_f32 v116, v122, v123\n"
    "v_mfma_f32_32x32x16_bf16 v[144:159], v[72:75], v[112:115], v[144:159]\n"
    "ds_write_b128 v212, v[136:139] offset:10240\n"
    "v_exp_f32 v120, v108\n"
    "v_exp_f32 v121, v109\n"
    "v_cvt_pk_bf16_f32 v117, v124, v125\n"
    "v_exp_f32 v122, v110\n"
    "v_exp_f32 v123, v111\n"
    "v_cvt_pk_bf16_f32 v118, v120, v121\n"
    "v_mfma_f32_32x32x16_bf16 v[96:111], v[64:67], v[240:243], 0\n"
    "buffer_load_dwordx4 v206, s[20:23], s40 offen offset:2048 lds\n"
    "v_exp_f32 v124, v80\n"
    "v_exp_f32 v125, v81\n"
    "v_cvt_pk_bf16_f32 v119, v122, v123\n"
    "v_exp_f32 v120, v82\n"
    "v_exp_f32 v121, v83\n"
    "v_cvt_pk_bf16_f32 v112, v124, v125\n"
    "v_mfma_f32_32x32x16_bf16 v[144:159], v[76:79], v[116:119], v[144:159]\n"
    "ds_write_b128 v212, v[140:143] offset:11264\n"
    "v_exp_f32 v122, v84\n"
    "v_exp_f32 v123, v85\n"
    "v_cvt_pk_bf16_f32 v113, v120, v121\n"
    "v_exp_f32 v124, v86\n"
    "v_exp_f32 v125, v87\n"
    "v_cvt_pk_bf16_f32 v114, v122, v123\n"
    "v_mfma_f32_32x32x16_bf16 v[96:111], v[68:71], v[244:247], v[96:111]\n"
    "buffer_load_dwordx4 a[220:223], v13, s[32:35], s1 offen offset:1024 sc1\n"
    "v_exp_f32 v120, v88\n"
    "v_exp_f32 v121, v89\n"
    "v_cvt_pk_bf16_f32 v115, v124, v125\n"
    "v_exp_f32 v122, v90\n"
    "v_exp_f32 v123, v91\n"
    "v_cvt_pk_bf16_f32 v116, v120, v121\n"
    "v_mfma_f32_32x32x16_bf16 v[160:175], v[56:59], v[112:115], v[160:175]\n"
    "ds_write_b128 v213, v[144:147] offset:12288\n"
    "v_exp_f32 v124, v92\n"
    "v_exp_f32 v125, v93\n"
    "v_cvt_pk_bf16_f32 v117, v122, v123\n"
    "v_exp_f32 v120, v94\n"
    "v_exp_f32 v121, v95\n"
    "v_cvt_pk_bf16_f32 v118, v124, v125\n"
    "v_mfma_f32_32x32x16_bf16 v[80:95], v[16:19], v[248:251], 0\n"
    "buffer_load_dwordx4 v207, s[20:23], s40 offen offset:3072 lds\n"
    "v_exp_f32 v122, v96\n"
    "v_exp_f32 v123, v97\n"
    "v_cvt_pk_bf16_f32 v119, v120, v121\n"
    "v_exp_f32 v124, v98\n"
    "v_exp_f32 v125, v99\n"
    "v_cvt_pk_bf16_f32 v112, v122, v123\n"
    "v_mfma_f32_32x32x16_bf16 v[160:175], v[60:63], v[116:119], v[160:175]\n"
    "ds_write_b128 v213, v[148:151] offset:13312\n"
    "v_exp_f32 v120, v100\n"
    "v_exp_f32 v121, v101\n"
    "v_cvt_pk_bf16_f32 v113, v124, v125\n"
    "v_exp_f32 v122, v102\n"
    "v_exp_f32 v123, v103\n"
    "v_cvt_pk_bf16_f32 v114, v120, v121\n"
    "v_mfma_f32_32x32x16_bf16 v[80:95], v[20:23], v[252:255], v[80:95]\n"
    "buffer_load_dwordx4 v[2:5], v199, s[4:7], s59 offen\n"
    "v_exp_f32 v124, v104\n"
    "v_exp_f32 v125, v105\n"
    "v_cvt_pk_bf16_f32 v115, v122, v123\n"
    "v_exp_f32 v120, v106\n"
    "v_exp_f32 v121, v107\n"
    "v_cvt_pk_bf16_f32 v116, v124, v125\n"
    "v_mfma_f32_32x32x16_bf16 v[160:175], v[72:75], v[112:115], v[160:175]\n"
    "ds_write_b128 v213, v[152:155] offset:14336\n"
    "v_exp_f32 v122, v108\n"
    "v_exp_f32 v123, v109\n"
    "v_cvt_pk_bf16_f32 v117, v120, v121\n"
    "v_exp_f32 v124, v110\n"
    "v_exp_f32 v125, v111\n"
    "v_cvt_pk_bf16_f32 v118, v122, v123\n"
    "v_mfma_f32_32x32x16_bf16 v[96:111], v[32:35], v[248:251], 0\n"
    "buffer_load_dwordx4 v[6:9], v199, s[4:7], s62 offen\n"
    "v_exp_f32 v120, v80\n"
    "v_exp_f32 v121, v81\n"
    "v_cvt_pk_bf16_f32 v119, v124, v125\n"
    "v_exp_f32 v122, v82\n"
    "v_exp_f32 v123, v83\n"
    "v_cvt_pk_bf16_f32 v112, v120, v121\n"
    "v_mfma_f32_32x32x16_bf16 v[160:175], v[76:79], v[116:119], v[160:175]\n"
    "ds_write_b128 v213, v[156:159] offset:15360\n"
    "v_exp_f32 v124, v84\n"
    "v_exp_f32 v125, v85\n"
    "v_cvt_pk_bf16_f32 v113, v122, v123\n"
    "v_exp_f32 v120, v86\n"
    "v_exp_f32 v121, v87\n"
    "v_cvt_pk_bf16_f32 v114, v124, v125\n"
    "v_mfma_f32_32x32x16_bf16 v[96:111], v[36:39], v[252:255], v[96:111]\n"
    "v_exp_f32 v122, v88\n"
    "v_exp_f32 v123, v89\n"
    "v_cvt_pk_bf16_f32 v115, v120, v121\n"
    "v_exp_f32 v124, v90\n"
    "v_exp_f32 v125, v91\n"
    "v_cvt_pk_bf16_f32 v116, v122, v123\n"
    "v_mfma_f32_32x32x16_bf16 v[176:191], v[24:27], v[112:115], v[176:191]\n"
    "ds_write_b128 v214, v[160:163] offset:16384\n"
    "v_exp_f32 v120, v92\n"
    "v_exp_f32 v121, v93\n"
    "v_cvt_pk_bf16_f32 v117, v124, v125\n"
    "v_exp_f32 v122, v94\n"
    "v_exp_f32 v123, v95\n"
    "v_cvt_pk_bf16_f32 v118, v120, v121\n"
    "v_mfma_f32_32x32x16_bf16 v[80:95], v[48:51], v[248:251], 0\n"
    "ds_write_b128 v214, v[164:167] offset:17408\n"
    "v_exp_f32 v124, v96\n"
    "v_exp_f32 v125, v97\n"
    "v_cvt_pk_bf16_f32 v119, v122, v123\n"
    "v_exp_f32 v120, v98\n"
    "v_exp_f32 v121, v99\n"
    "v_cvt_pk_bf16_f32 v112, v124, v125\n"
    "v_mfma_f32_32x32x16_bf16 v[176:191], v[28:31], v[116:119], v[176:191]\n"
    "ds_write_b128 v214, v[168:171] offset:18432\n"
    "v_exp_f32 v122, v100\n"
    "v_exp_f32 v123, v101\n"
    "v_cvt_pk_bf16_f32 v113, v120, v121\n"
    "v_exp_f32 v124, v102\n"
    "v_exp_f32 v125, v103\n"
    "v_cvt_pk_bf16_f32 v114, v122, v123\n"
    "v_mfma_f32_32x32x16_bf16 v[80:95], v[52:55], v[252:255], v[80:95]\n"
    "ds_write_b128 v214, v[172:175] offset:19456\n"
    "v_exp_f32 v120, v104\n"
    "v_exp_f32 v121, v105\n"
    "v_cvt_pk_bf16_f32 v115, v124, v125\n"
    "v_exp_f32 v122, v106\n"
    "v_exp_f32 v123, v107\n"
    "v_cvt_pk_bf16_f32 v116, v120, v121\n"
    "v_mfma_f32_32x32x16_bf16 v[176:191], v[40:43], v[112:115], v[176:191]\n"
    "v_exp_f32 v124, v108\n"
    "v_exp_f32 v125, v109\n"
    "v_cvt_pk_bf16_f32 v117, v122, v123\n"
    "v_exp_f32 v120, v110\n"
    "v_exp_f32 v121, v111\n"
    "v_cvt_pk_bf16_f32 v118, v124, v125\n"
    "v_mfma_f32_32x32x16_bf16 v[96:111], v[64:67], v[248:251], 0\n"
    "s_waitcnt lgkmcnt(0)\n"
    "v_exp_f32 v122, v80\n"
    "v_exp_f32 v123, v81\n"
    "v_cvt_pk_bf16_f32 v119, v120, v121\n"
    "v_exp_f32 v124, v82\n"
    "v_exp_f32 v125, v83\n"
    "v_cvt_pk_bf16_f32 v112, v122, v123\n"
    "v_mfma_f32_32x32x16_bf16 v[176:191], v[44:47], v[116:119], v[176:191]\n"
    "s_barrier\n"
    "v_exp_f32 v120, v84\n"
    "v_exp_f32 v121, v85\n"
    "v_cvt_pk_bf16_f32 v113, v124, v125\n"
    "v_exp_f32 v122, v86\n"
    "v_exp_f32 v123, v87\n"
    "v_cvt_pk_bf16_f32 v114, v120, v121\n"
    "v_mfma_f32_32x32x16_bf16 v[96:111], v[68:71], v[252:255], v[96:111]\n"
    "ds_read_b128 v[128:131], v215 offset:8192\n"
    "ds_read_b128 v[132:135], v215 offset:9216\n"
    "v_exp_f32 v124, v88\n"
    "v_exp_f32 v125, v89\n"
    "v_cvt_pk_bf16_f32 v115, v122, v123\n"
    "v_exp_f32 v120, v90\n"
    "v_exp_f32 v121, v91\n"
    "v_cvt_pk_bf16_f32 v116, v124, v125\n"
    "v_mfma_f32_32x32x16_bf16 v[176:191], v[56:59], v[112:115], v[176:191]\n"
    "ds_read_b128 v[136:139], v215 offset:10240\n"
    "ds_read_b128 v[140:143], v215 offset:11264\n"
    "v_exp_f32 v122, v92\n"
    "v_exp_f32 v123, v93\n"
    "v_cvt_pk_bf16_f32 v117, v120, v121\n"
    "v_exp_f32 v124, v94\n"
    "v_exp_f32 v125, v95\n"
    "v_cvt_pk_bf16_f32 v118, v122, v123\n"
    "ds_read_b128 v[144:147], v215 offset:12288\n"
    "ds_read_b128 v[148:151], v215 offset:13312\n"
    "v_exp_f32 v120, v96\n"
    "v_exp_f32 v121, v97\n"
    "v_cvt_pk_bf16_f32 v119, v124, v125\n"
    "v_exp_f32 v122, v98\n"
    "v_exp_f32 v123, v99\n"
    "v_cvt_pk_bf16_f32 v112, v120, v121\n"
    "v_mfma_f32_32x32x16_bf16 v[176:191], v[60:63], v[116:119], v[176:191]\n"
    "ds_read_b128 v[152:155], v215 offset:14336\n"
    "ds_read_b128 v[156:159], v215 offset:15360\n"
    "v_exp_f32 v124, v100\n"
    "v_exp_f32 v125, v101\n"
    "v_cvt_pk_bf16_f32 v113, v122, v123\n"
    "v_exp_f32 v120, v102\n"
    "v_exp_f32 v121, v103\n"
    "v_cvt_pk_bf16_f32 v114, v124, v125\n"
    "ds_read_b128 v[160:163], v215 offset:16384\n"
    "ds_read_b128 v[164:167], v215 offset:17408\n"
    "v_exp_f32 v122, v104\n"
    "v_exp_f32 v123, v105\n"
    "v_cvt_pk_bf16_f32 v115, v120, v121\n"
    "v_exp_f32 v124, v106\n"
    "v_exp_f32 v125, v107\n"
    "v_cvt_pk_bf16_f32 v116, v122, v123\n"
    "v_mfma_f32_32x32x16_bf16 v[176:191], v[72:75], v[112:115], v[176:191]\n"
    "ds_read_b128 v[168:171], v215 offset:18432\n"
    "ds_read_b128 v[172:175], v215 offset:19456\n"
    "v_exp_f32 v120, v108\n"
    "v_exp_f32 v121, v109\n"
    "v_cvt_pk_bf16_f32 v117, v124, v125\n"
    "v_exp_f32 v122, v110\n"
    "v_exp_f32 v123, v111\n"
    "v_cvt_pk_bf16_f32 v118, v120, v121\n"
    "v_cvt_pk_bf16_f32 v119, v122, v123\n"
    "s_add_u32 s66, s66, 98304\n"
    "s_add_u32 s40, s40, 28672\n"
    "s_add_u32 m0, s78, 8192\n"
    "s_waitcnt lgkmcnt(4)\n"
    "v_mfma_f32_32x32x16_bf16 v[176:191], v[76:79], v[116:119], v[176:191]\n"
    "buffer_load_dwordx4 v204, s[20:23], s40 offen lds\n"
    "; ---- merge the four key ranges: wave w keeps query tile w\n"
    "v_add_f32 v128, v128, v144\n"
    "v_add_f32 v129, v129, v145\n"
    "v_add_f32 v130, v130, v146\n"
    "v_add_f32 v131, v131, v147\n"
    "v_add_f32 v132, v132, v148\n"
    "v_add_f32 v133, v133, v149\n"
    "v_add_f32 v134, v134, v150\n"
    "v_add_f32 v135, v135, v151\n"
    "v_add_f32 v136, v136, v152\n"
    "v_add_f32 v137, v137, v153\n"
    "v_add_f32 v138, v138, v154\n"
    "v_add_f32 v139, v139, v155\n"
    "v_add_f32 v140, v140, v156\n"
    "v_add_f32 v141, v141, v157\n"
    "v_add_f32 v142, v142, v158\n"
    "v_add_f32 v143, v143, v159\n"
    "buffer_load_dwordx4 v205, s[20:23], s40 offen offset:1024 lds\n"
    "s_waitcnt lgkmcnt(0)\n"
    "v_pk_add_f32 v[188:189], v[188:189], v[172:173]\n"
    "v_pk_add_f32 v[188:189], v[188:189], v[140:141]\n"
    "v_mov_b32 v192, v188\n"
    "v_mov_b32 v194, v188\n"
    "v_pk_add_f32 v[190:191], v[190:191], v[174:175]\n"
    "v_pk_add_f32 v[190:191], v[190:191], v[142:143]\n"
    "v_permlane32_swap_b32 v194, v192\n"
    "s_mov_b32 exec_lo, 0\n"
    "v_mov_b32 v188, 0\n"
    "s_mov_b32 exec_lo, -1\n"
    "v_rcp_f32 v192, v192\n"
    "v_pk_add_f32 v[176:177], v[176:177], v[160:161]\n"
    "v_pk_add_f32 v[176:177], v[176:177], v[128:129]\n"
    "buffer_load_dwordx4 v206, s[20:23], s40 offen offset:2048 lds\n"
    "v_pk_add_f32 v[178:179], v[178:179], v[162:163]\n"
    "v_pk_add_f32 v[178:179], v[178:179], v[130:131]\n"
    "v_pk_add_f32 v[180:181], v[180:181], v[164:165]\n"
    "v_pk_add_f32 v[180:181], v[180:181], v[132:133]\n"
    "v_pk_add_f32 v[182:183], v[182:183], v[166:167]\n"
    "v_pk_add_f32 v[182:183], v[182:183], v[134:135]\n"
    "v_pk_add_f32 v[184:185], v[184:185], v[168:169]\n"
    "v_pk_add_f32 v[184:185], v[184:185], v[136:137]\n"
    "v_pk_add_f32 v[186:187], v[186:187], v[170:171]\n"
    "v_pk_add_f32 v[186:187], v[186:187], v[138:139]\n"
    "v_pk_mul_f32 v[176:177], v[176:177], v[192:193] op_sel_hi:[1,0]\n"
    "v_pk_mul_f32 v[178:179], v[178:179], v[192:193] op_sel_hi:[1,0]\n"
    "v_pk_mul_f32 v[180:181], v[180:181], v[192:193] op_sel_hi:[1,0]\n"
    "v_pk_mul_f32 v[182:183], v[182:183], v[192:193] op_sel_hi:[1,0]\n"
    "v_pk_mul_f32 v[184:185], v[184:185], v[192:193] op_sel_hi:[1,0]\n"
    "v_pk_mul_f32 v[186:187], v[186:187], v[192:193] op_sel_hi:[1,0]\n"
    "v_pk_mul_f32 v[188:189], v[188:189], v[192:193] op_sel_hi:[1,0]\n"
    "v_pk_mul_f32 v[190:191], v[190:191], v[192:193] op_sel_hi:[1,0]\n"
    "v_cvt_pk_bf16_f32 v128, v176, v177\n"
    "v_cvt_pk_bf16_f32 v129, v178, v179\n"
    "v_cvt_pk_bf16_f32 v130, v184, v185\n"
    "v_cvt_pk_bf16_f32 v131, v186, v187\n"
    "buffer_store_dwordx4 v[128:131], v223, s[8:11], s58 offen sc1\n"
    "v_cvt_pk_bf16_f32 v132, v180, v181\n"
    "v_cvt_pk_bf16_f32 v133, v182, v183\n"
    "v_cvt_pk_bf16_f32 v134, v188, v189\n"
    "v_cvt_pk_bf16_f32 v135, v190, v191\n"
    "buffer_store_dwordx4 v[132:135], v223, s[8:11], s58 offen offset:512 sc1\n"
    "s_cmp_ge_u32 s88, 7\n"
    "s_cbranch_scc1 .Lo_33_%=\n"
    ".Lr_34_%=:\n"
    "; ---- phase 3: 64 x 64 output tile, K split over the waves\n"
    "v_mfma_f32_16x16x32_bf16 v[16:19], a[224:227], a[0:3], 0\n"
    "ds_read_b64_tr_b16 v[224:225], v208 offset:36864\n"
    "v_mfma_f32_16x16x32_bf16 v[16:19], a[228:231], a[16:19], v[16:19]\n"
    "ds_read_b64_tr_b16 v[226:227], v208 offset:38912\n"
    "v_mfma_f32_16x16x32_bf16 v[32:35], a[224:227], a[4:7], 0\n"
    "ds_read_b64_tr_b16 v[228:229], v208 offset:28672\n"
    "v_mfma_f32_16x16x32_bf16 v[32:35], a[228:231], a[20:23], v[32:35]\n"
    "buffer_load_dwordx4 v207, s[20:23], s40 offen offset:3072 lds\n"
    "v_mfma_f32_16x16x32_bf16 v[48:51], a[224:227], a[8:11], 0\n"
    "ds_read_b64_tr_b16 v[230:231], v208 offset:30720\n"
    "s_add_u32 m0, s78, 12288\n"
    "v_mfma_f32_16x16x32_bf16 v[48:51], a[228:231], a[24:27], v[48:51]\n"
    "ds_read_b64_tr_b16 v[232:233], v209 offset:36864\n"
    "v_mfma_f32_16x16x32_bf16 v[64:67], a[224:227], a[12:15], 0\n"
    "ds_read_b64_tr_b16 v[234:235], v209 offset:38912\n"
    "v_mfma_f32_16x16x32_bf16 v[64:67], a[228:231], a[28:31], v[64:67]\n"
    "buffer_load_dwordx4 v204, s[20:23], s41 offen lds\n"
    "v_mfma_f32_16x16x32_bf16 v[20:23], a[232:235], a[0:3], 0\n"
    "ds_read_b64_tr_b16 v[236:237], v209 offset:28672\n"
    "v_add_u32 v10, s61, v1\n"
    "v_mfma_f32_16x16x32_bf16 v[20:23], a[236:239], a[16:19], v[20:23]\n"
    "ds_read_b64_tr_b16 v[238:239], v209 offset:30720\n"
    "v_xor_b32 v11, 16384, v10\n"
    "v_mfma_f32_16x16x32_bf16 v[36:39], a[232:235], a[4:7], 0\n"
    "ds_read_b64_tr_b16 v[240:241], v210 offset:36864\n"
    "v_xor_b32 v12, 32768, v10\n"
    "v_mfma_f32_16x16x32_bf16 v[36:39], a[236:239], a[20:23], v[36:39]\n"
    "buffer_load_dwordx4 v205, s[20:23], s41 offen offset:1024 lds\n"
    "v_mfma_f32_16x16x32_bf16 v[52:55], a[232:235], a[8:11], 0\n"
    "ds_read_b64_tr_b16 v[242:243], v210 offset:38912\n"
    "v_xor_b32 v13, 49152, v10\n"
    "v_mfma_f32_16x16x32_bf16 v[52:55], a[236:239], a[24:27], v[52:55]\n"
    "ds_read_b64_tr_b16 v[244:245], v210 offset:28672\n"
    "v_and_b32 v108, 7, v0\n"
    "v_mfma_f32_16x16x32_bf16 v[68:71], a[232:235], a[12:15], 0\n"
    "ds_read_b64_tr_b16 v[246:247], v210 offset:30720\n"
    "v_and_b32 v109, 4, v0\n"
    "v_mfma_f32_16x16x32_bf16 v[68:71], a[236:239], a[28:31], v[68:71]\n"
    "v_lshl_add_u32 v108, v109, 1, v108\n"
    "v_mfma_f32_16x16x32_bf16 v[24:27], a[240:243], a[0:3], 0\n"
    "ds_read_b64_tr_b16 v[248:249], v211 offset:36864\n"
    "v_lshrrev_b32 v109, 4, v0\n"
    "v_mfma_f32_16x16x32_bf16 v[24:27], a[244:247], a[16:19], v[24:27]\n"
    "ds_read_b64_tr_b16 v[250:251], v211 offset:38912\n"
    "v_xor_b32 v108, v108, v109\n"
    "v_mfma_f32_16x16x32_bf16 v[40:43], a[240:243], a[4:7], 0\n"
    "ds_read_b64_tr_b16 v[252:253], v211 offset:28672\n"
    "v_and_b32 v109, 15, v0\n"
    "v_mfma_f32_16x16x32_bf16 v[40:43], a[244:247], a[20:23], v[40:43]\n"
    "buffer_load_dwordx4 v206, s[20:23], s41 offen offset:2048 lds\n"
    "v_mfma_f32_16x16x32_bf16 v[56:59], a[240:243], a[8:11], 0\n"
    "ds_read_b64_tr_b16 v[254:255], v211 offset:30720\n"
    ".Lpage_7_%=:\n"
    "s_cbranch_execz .Lpage_0_%=\n"
    "v_mfma_f32_16x16x32_bf16 v[56:59], a[244:247], a[24:27], v[56:59]\n"
    "v_lshlrev_b32 v109, 8, v109\n"
    "v_mfma_f32_16x16x32_bf16 v[72:75], a[240:243], a[12:15], 0\n"
    "v_lshl_add_u32 v108, v108, 4, v109\n"
    "v_mfma_f32_16x16x32_bf16 v[72:75], a[244:247], a[28:31], v[72:75]\n"
    "buffer_load_dwordx4 v207, s[20:23], s41 offen offset:3072 lds\n"
    "v_mfma_f32_16x16x32_bf16 v[28:31], a[248:251], a[0:3], 0\n"
    "ds_read_b64_tr_b16 a[224:225], v208 offset:32768\n"
    "s_add_u32 s41, s41, 28672\n"
    "v_mfma_f32_16x16x32_bf16 v[28:31], a[252:255], a[16:19], v[28:31]\n"
    "s_add_u32 m0, s78, 16384\n"
    "v_mfma_f32_16x16x32_bf16 v[44:47], a[248:251], a[4:7], 0\n"
    "ds_read_b64_tr_b16 a[226:227], v208 offset:34816\n"
    "s_waitcnt lgkmcnt(2)\n"
    "v_mfma_f32_16x16x32_bf16 v[44:47], a[252:255], a[20:23], v[44:47]\n"
    "buffer_load_dwordx4 v204, s[20:23], s41 offen lds\n"
    "v_mfma_f32_16x16x32_bf16 v[60:63], a[248:251], a[8:11], 0\n"
    "ds_read_b64_tr_b16 a[228:229], v208\n"
    "v_add_u32 v96, s78, v108\n"
    "v_mfma_f32_16x16x32_bf16 v[60:63], a[252:255], a[24:27], v[60:63]\n"
    "v_xor_b32 v97, 64, v96\n"
    "v_mfma_f32_16x16x32_bf16 v[76:79], a[248:251], a[12:15], 0\n"
    "ds_read_b64_tr_b16 a[230:231], v208 offset:2048\n"
    "v_mfma_f32_16x16x32_bf16 v[76:79], a[252:255], a[28:31], v[76:79]\n"
    "v_xor_b32 v98, 128, v96\n"
    "v_mfma_f32_16x16x32_bf16 v[16:19], v[224:227], a[32:35], v[16:19]\n"
    "ds_read_b64_tr_b16 a[232:233], v209 offset:32768\n"
    "v_xor_b32 v99, 192, v96\n"
    "v_mfma_f32_16x16x32_bf16 v[16:19], v[228:231], a[48:51], v[16:19]\n"
    "buffer_load_dwordx4 v205, s[20:23], s41 offen offset:1024 lds\n"
    "v_mfma_f32_16x16x32_bf16 v[32:35], v[224:227], a[36:39], v[32:35]\n"
    "ds_read_b64_tr_b16 a[234:235], v209 offset:34816\n"
    "v_lshrrev_b32 v110, 3, v0\n"
    "v_mfma_f32_16x16x32_bf16 v[32:35], v[228:231], a[52:55], v[32:35]\n"
    "v_and_b32 v108, 7, v110\n"
    "v_mfma_f32_16x16x32_bf16 v[48:51], v[224:227], a[40:43], v[48:51]\n"
    "ds_read_b64_tr_b16 a[236:237], v209\n"
    "v_and_b32 v109, 4, v110\n"
    "v_mfma_f32_16x16x32_bf16 v[48:51], v[228:231], a[56:59], v[48:51]\n"
    "v_lshl_add_u32 v108, v109, 1, v108\n"
    "v_mfma_f32_16x16x32_bf16 v[64:67], v[224:227], a[44:47], v[64:67]\n"
    "ds_read_b64_tr_b16 a[238:239], v209 offset:2048\n"
    "v_and_b32 v109, 7, v0\n"
    "v_mfma_f32_16x16x32_bf16 v[64:67], v[228:231], a[60:63], v[64:67]\n"
    "buffer_load_dwordx4 v206, s[20:23], s41 offen offset:2048 lds\n"
    "v_mfma_f32_16x16x32_bf16 v[20:23], v[232:235], a[32:35], v[20:23]\n"
    "ds_read_b64_tr_b16 a[240:241], v210 offset:32768\n"
    "s_waitcnt vmcnt(8)\n"
    "v_mfma_f32_16x16x32_bf16 v[20:23], v[236:239], a[48:51], v[20:23]\n"
    "s_barrier\n"
    "v_mfma_f32_16x16x32_bf16 v[36:39], v[232:235], a[36:39], v[36:39]\n"
    "ds_read_b64_tr_b16 a[242:243], v210 offset:34816\n"
    "v_lshlrev_b32 v109, 1, v109\n"
    "v_mfma_f32_16x16x32_bf16 v[36:39], v[236:239], a[52:55], v[36:39]\n"
    "buffer_store_dword v219, v216, s[8:11], s66 offen sc1\n"
    "v_mfma_f32_16x16x32_bf16 v[52:55], v[232:235], a[40:43], v[52:55]\n"
    "ds_read_b64_tr_b16 a[244:245], v210\n"
    "v_xor_b32 v108, v108, v109\n"
    "v_mfma_f32_16x16x32_bf16 v[52:55], v[236:239], a[56:59], v[52:55]\n"
    "v_lshlrev_b32 v110, 8, v110\n"
    "v_mfma_f32_16x16x32_bf16 v[68:71], v[232:235], a[44:47], v[68:71]\n"
    "ds_read_b64_tr_b16 a[246:247], v210 offset:2048\n"
    "v_lshl_add_u32 v108, v108, 4, v110\n"
    "v_mfma_f32_16x16x32_bf16 v[68:71], v[236:239], a[60:63], v[68:71]\n"
    "buffer_load_dwordx4 v207, s[20:23], s41 offen offset:3072 lds\n"
    "v_mfma_f32_16x16x32_bf16 v[24:27], v[240:243], a[32:35], v[24:27]\n"
    "ds_read_b64_tr_b16 a[248:249], v211 offset:32768\n"
    "v_mfma_f32_16x16x32_bf16 v[24:27], v[244:247], a[48:51], v[24:27]\n"
    "s_add_u32 m0, s78, 36864\n"
    "v_mfma_f32_16x16x32_bf16 v[40:43], v[240:243], a[36:39], v[40:43]\n"
    "ds_read_b64_tr_b16 a[250:251], v211 offset:34816\n"
    "v_lshlrev_b32 v80, 16, v2\n"
    "v_mfma_f32_16x16x32_bf16 v[40:43], v[244:247], a[52:55], v[40:43]\n"
    "buffer_load_dwordx4 v204, s[20:23], s42 offen lds\n"
    "v_mfma_f32_16x16x32_bf16 v[56:59], v[240:243], a[40:43], v[56:59]\n"
    "ds_read_b64_tr_b16 a[252:253], v211\n"
    "v_mfma_f32_16x16x32_bf16 v[56:59], v[244:247], a[56:59], v[56:59]\n"
    "v_and_b32 v81, 0xffff0000, v2\n"
    "v_mfma_f32_16x16x32_bf16 v[72:75], v[240:243], a[44:47], v[72:75]\n"
    "ds_read_b64_tr_b16 a[254:255], v211 offset:2048\n"
    "v_lshlrev_b32 v82, 16, v3\n"
    "v_mfma_f32_16x16x32_bf16 v[72:75], v[244:247], a[60:63], v[72:75]\n"
    "buffer_load_dwordx4 v205, s[20:23], s42 offen offset:1024 lds\n"
    "v_mfma_f32_16x16x32_bf16 v[28:31], v[248:251], a[32:35], v[28:31]\n"
    "ds_read_b64_tr_b16 a[0:1], v208 offset:4096\n"
    "v_and_b32 v83, 0xffff0000, v3\n"
    "v_mfma_f32_16x16x32_bf16 v[28:31], v[252:255], a[48:51], v[28:31]\n"
    "v_lshlrev_b32 v84, 16, v4\n"
    "v_mfma_f32_16x16x32_bf16 v[44:47], v[248:251], a[36:39], v[44:47]\n"
    "ds_read_b64_tr_b16 a[2:3], v208 offset:6144\n"
    "v_and_b32 v85, 0xffff0000, v4\n"
    "v_mfma_f32_16x16x32_bf16 v[44:47], v[252:255], a[52:55], v[44:47]\n"
    "v_lshlrev_b32 v86, 16, v5\n"
    "v_mfma_f32_16x16x32_bf16 v[60:63], v[248:251], a[40:43], v[60:63]\n"
    "ds_read_b64_tr_b16 a[4:5], v208 offset:20480\n"
    "s_waitcnt lgkmcnt(3)\n"
    "v_mfma_f32_16x16x32_bf16 v[60:63], v[252:255], a[56:59], v[60:63]\n"
    "buffer_load_dwordx4 v206, s[20:23], s42 offen offset:2048 lds\n"
    "v_mfma_f32_16x16x32_bf16 v[76:79], v[248:251], a[44:47], v[76:79]\n"
    "ds_read_b64_tr_b16 a[6:7], v208 offset:22528\n"
    "v_mfma_f32_16x16x32_bf16 v[76:79], v[252:255], a[60:63], v[76:79]\n"
    "v_and_b32 v87, 0xffff0000, v5\n"
    "v_mfma_f32_16x16x32_bf16 v[16:19], a[224:227], a[64:67], v[16:19]\n"
    "ds_read_b64_tr_b16 a[8:9], v209 offset:4096\n"
    "v_lshlrev_b32 v88, 16, v6\n"
    "v_mfma_f32_16x16x32_bf16 v[16:19], a[228:231], a[80:83], v[16:19]\n"
    "buffer_load_dwordx4 v207, s[20:23], s42 offen offset:3072 lds\n"
    "v_mfma_f32_16x16x32_bf16 v[32:35], a[224:227], a[68:71], v[32:35]\n"
    "ds_read_b64_tr_b16 a[10:11], v209 offset:6144\n"
    "s_add_u32 s42, s42, 28672\n"
    "v_mfma_f32_16x16x32_bf16 v[32:35], a[228:231], a[84:87], v[32:35]\n"
    "s_add_u32 m0, s78, 28672\n"
    "v_mfma_f32_16x16x32_bf16 v[48:51], a[224:227], a[72:75], v[48:51]\n"
    "ds_read_b64_tr_b16 a[12:13], v209 offset:20480\n"
    "v_and_b32 v89, 0xffff0000, v6\n"
    "v_mfma_f32_16x16x32_bf16 v[48:51], a[228:231], a[88:91], v[48:51]\n"
    "v_lshlrev_b32 v90, 16, v7\n"
    "v_mfma_f32_16x16x32_bf16 v[64:67], a[224:227], a[76:79], v[64:67]\n"
    "ds_read_b64_tr_b16 a[14:15], v209 offset:22528\n"
    "v_and_b32 v91, 0xffff0000, v7\n"
    "v_mfma_f32_16x16x32_bf16 v[64:67], a[228:231], a[92:95], v[64:67]\n"
    "buffer_load_dwordx4 v204, s[20:23], s42 offen lds\n"
    "v_mfma_f32_16x16x32_bf16 v[20:23], a[232:235], a[64:67], v[20:23]\n"
    "ds_read_b64_tr_b16 a[16:17], v210 offset:4096\n"
    "v_lshlrev_b32 v92, 16, v8\n"
    "v_mfma_f32_16x16x32_bf16 v[20:23], a[236:239], a[80:83], v[20:23]\n"
    "v_and_b32 v93, 0xffff0000, v8\n"
    "v_mfma_f32_16x16x32_bf16 v[36:39], a[232:235], a[68:71], v[36:39]\n"
    "ds_read_b64_tr_b16 a[18:19], v210 offset:6144\n"
    "v_lshlrev_b32 v94, 16, v9\n"
    "v_mfma_f32_16x16x32_bf16 v[36:39], a[236:239], a[84:87], v[36:39]\n"
    "v_and_b32 v95, 0xffff0000, v9\n"
    "v_mfma_f32_16x16x32_bf16 v[52:55], a[232:235], a[72:75], v[52:55]\n"
    "ds_read_b64_tr_b16 a[20:21], v210 offset:20480\n"
    "v_sub_u32 v108, v108, v1\n"
    "v_mfma_f32_16x16x32_bf16 v[52:55], a[236:239], a[88:91], v[52:55]\n"
    "buffer_load_dwordx4 v205, s[20:23], s42 offen offset:1024 lds\n"
    "v_mfma_f32_16x16x32_bf16 v[68:71], a[232:235], a[76:79], v[68:71]\n"
    "ds_read_b64_tr_b16 a[22:23], v210 offset:22528\n"
    "v_add_u32 v224, v212, v108\n"
    "v_mfma_f32_16x16x32_bf16 v[68:71], a[236:239], a[92:95], v[68:71]\n"
    "v_xor_b32 v225, 16, v224\n"
    "v_mfma_f32_16x16x32_bf16 v[24:27], a[240:243], a[64:67], v[24:27]\n"
    "ds_read_b64_tr_b16 a[24:25], v211 offset:4096\n"
    "v_add_u32 v228, v213, v108\n"
    "v_mfma_f32_16x16x32_bf16 v[24:27], a[244:247], a[80:83], v[24:27]\n"
    "v_xor_b32 v229, 16, v228\n"
    "v_mfma_f32_16x16x32_bf16 v[40:43], a[240:243], a[68:71], v[40:43]\n"
    "ds_read_b64_tr_b16 a[26:27], v211 offset:6144\n"
    "v_add_u32 v232, v214, v108\n"
    "v_mfma_f32_16x16x32_bf16 v[40:43], a[244:247], a[84:87], v[40:43]\n"
    "buffer_load_dwordx4 v206, s[20:23], s42 offen offset:2048 lds\n"
    "v_mfma_f32_16x16x32_bf16 v[56:59], a[240:243], a[72:75], v[56:59]\n"
    "ds_read_b64_tr_b16 a[28:29], v211 offset:20480\n"
    "s_waitcnt vmcnt(16)\n"
    "v_mfma_f32_16x16x32_bf16 v[56:59], a[244:247], a[88:91], v[56:59]\n"
    "v_xor_b32 v233, 16, v232\n"
    "v_mfma_f32_16x16x32_bf16 v[72:75], a[240:243], a[76:79], v[72:75]\n"
    "ds_read_b64_tr_b16 a[30:31], v211 offset:22528\n"
    "v_add_u32 v236, v215, v108\n"
    "v_mfma_f32_16x16x32_bf16 v[72:75], a[244:247], a[92:95], v[72:75]\n"
    "buffer_load_dwordx4 v207, s[20:23], s42 offen offset:3072 lds\n"
    "v_mfma_f32_16x16x32_bf16 v[28:31], a[248:251], a[64:67], v[28:31]\n"
    "ds_read_b64_tr_b16 a[224:225], v208 offset:24576\n"
    "v_xor_b32 v237, 16, v236\n"
    "v_mfma_f32_16x16x32_bf16 v[28:31], a[252:255], a[80:83], v[28:31]\n"
    "v_subrev_u32 v204, s56, v204\n"
    "v_mfma_f32_16x16x32_bf16 v[44:47], a[248:251], a[68:71], v[44:47]\n"
    "ds_read_b64_tr_b16 a[226:227], v208 offset:26624\n"
    "v_subrev_u32 v205, s56, v205\n"
    "v_mfma_f32_16x16x32_bf16 v[44:47], a[252:255], a[84:87], v[44:47]\n"
    "v_subrev_u32 v206, s56, v206\n"
    "v_mfma_f32_16x16x32_bf16 v[60:63], a[248:251], a[72:75], v[60:63]\n"
    "ds_read_b64_tr_b16 a[228:229], v208 offset:8192\n"
    "s_waitcnt lgkmcnt(3)\n"
    "v_mfma_f32_16x16x32_bf16 v[60:63], a[252:255], a[88:91], v[60:63]\n"
    "v_subrev_u32 v207, s56, v207\n"
    "v_mfma_f32_16x16x32_bf16 v[76:79], a[248:251], a[76:79], v[76:79]\n"
    "ds_read_b64_tr_b16 a[230:231], v208 offset:10240\n"
    "v_mfma_f32_16x16x32_bf16 v[76:79], a[252:255], a[92:95], v[76:79]\n"
    "s_add_u32 m0, s78, 32768\n"
    "v_mfma_f32_16x16x32_bf16 v[16:19], a[0:3], a[96:99], v[16:19]\n"
    "ds_read_b64_tr_b16 a[232:233], v209 offset:24576\n"
    "v_mfma_f32_16x16x32_bf16 v[16:19], a[4:7], a[112:115], v[16:19]\n"
    "buffer_load_dwordx4 v204, s[24:27], s57 offen lds\n"
    "v_mfma_f32_16x16x32_bf16 v[32:35], a[0:3], a[100:103], v[32:35]\n"
    "ds_read_b64_tr_b16 a[234:235], v209 offset:26624\n"
    "v_mfma_f32_16x16x32_bf16 v[32:35], a[4:7], a[116:119], v[32:35]\n"
    "v_mfma_f32_16x16x32_bf16 v[48:51], a[0:3], a[104:107], v[48:51]\n"
    "ds_read_b64_tr_b16 a[236:237], v209 offset:8192\n"
    "v_mfma_f32_16x16x32_bf16 v[48:51], a[4:7], a[120:123], v[48:51]\n"
    "buffer_load_dwordx4 v205, s[24:27], s57 offen offset:1024 lds\n"
    "v_mfma_f32_16x16x32_bf16 v[64:67], a[0:3], a[108:111], v[64:67]\n"
    "ds_read_b64_tr_b16 a[238:239], v209 offset:10240\n"
    "v_mfma_f32_16x16x32_bf16 v[64:67], a[4:7], a[124:127], v[64:67]\n"
    "v_mfma_f32_16x16x32_bf16 v[20:23], a[8:11], a[96:99], v[20:23]\n"
    "ds_read_b64_tr_b16 a[240:241], v210 offset:24576\n"
    "v_mfma_f32_16x16x32_bf16 v[20:23], a[12:15], a[112:115], v[20:23]\n"
    "buffer_load_dwordx4 v206, s[24:27], s57 offen offset:2048 lds\n"
    "v_mfma_f32_16x16x32_bf16 v[36:39], a[8:11], a[100:103], v[36:39]\n"
    "ds_read_b64_tr_b16 a[242:243], v210 offset:26624\n"
    "v_mfma_f32_16x16x32_bf16 v[36:39], a[12:15], a[116:119], v[36:39]\n"
    "v_mfma_f32_16x16x32_bf16 v[52:55], a[8:11], a[104:107], v[52:55]\n"
    "ds_read_b64_tr_b16 a[244:245], v210 offset:8192\n"
    "v_mfma_f32_16x16x32_bf16 v[52:55], a[12:15], a[120:123], v[52:55]\n"
    "buffer_load_dwordx4 v207, s[24:27], s57 offen offset:3072 lds\n"
    "v_mfma_f32_16x16x32_bf16 v[68:71], a[8:11], a[108:111], v[68:71]\n"
    "ds_read_b64_tr_b16 a[246:247], v210 offset:10240\n"
    "s_add_u32 s57, s57, 25088\n"
    "v_mfma_f32_16x16x32_bf16 v[68:71], a[12:15], a[124:127], v[68:71]\n"
    "s_add_u32 m0, s78, 0\n"
    "v_mfma_f32_16x16x32_bf16 v[24:27], a[16:19], a[96:99], v[24:27]\n"
    "ds_read_b64_tr_b16 a[248:249], v211 offset:24576\n"
    "v_mfma_f32_16x16x32_bf16 v[24:27], a[20:23], a[112:115], v[24:27]\n"
    "buffer_load_dwordx4 v204, s[24:27], s57 offen lds\n"
    "v_mfma_f32_16x16x32_bf16 v[40:43], a[16:19], a[100:103], v[40:43]\n"
    "ds_read_b64_tr_b16 a[250:251], v211 offset:26624\n"
    "v_mfma_f32_16x16x32_bf16 v[40:43], a[20:23], a[116:119], v[40:43]\n"
    "v_mfma_f32_16x16x32_bf16 v[56:59], a[16:19], a[104:107], v[56:59]\n"
    "ds_read_b64_tr_b16 a[252:253], v211 offset:8192\n"
    "s_waitcnt vmcnt(13)\n"
    "v_mfma_f32_16x16x32_bf16 v[56:59], a[20:23], a[120:123], v[56:59]\n"
    "buffer_load_dwordx4 v205, s[24:27], s57 offen offset:1024 lds\n"
    "v_mfma_f32_16x16x32_bf16 v[72:75], a[16:19], a[108:111], v[72:75]\n"
    "ds_read_b64_tr_b16 a[254:255], v211 offset:10240\n"
    "v_mfma_f32_16x16x32_bf16 v[72:75], a[20:23], a[124:127], v[72:75]\n"
    "v_mfma_f32_16x16x32_bf16 v[28:31], a[24:27], a[96:99], v[28:31]\n"
    "ds_read_b64_tr_b16 a[0:1], v208 offset:12288\n"
    "v_mfma_f32_16x16x32_bf16 v[28:31], a[28:31], a[112:115], v[28:31]\n"
    "v_mfma_f32_16x16x32_bf16 v[44:47], a[24:27], a[100:103], v[44:47]\n"
    "ds_read_b64_tr_b16 a[2:3], v208 offset:14336\n"
    "v_mfma_f32_16x16x32_bf16 v[44:47], a[28:31], a[116:119], v[44:47]\n"
    "buffer_load_dwordx4 v206, s[24:27], s57 offen offset:2048 lds\n"
    "v_mfma_f32_16x16x32_bf16 v[60:63], a[24:27], a[104:107], v[60:63]\n"
    "ds_read_b64_tr_b16 a[4:5], v208 offset:16384\n"
    "s_waitcnt lgkmcnt(3)\n"
    "v_mfma_f32_16x16x32_bf16 v[60:63], a[28:31], a[120:123], v[60:63]\n"
    "v_mfma_f32_16x16x32_bf16 v[76:79], a[24:27], a[108:111], v[76:79]\n"
    "ds_read_b64_tr_b16 a[6:7], v208 offset:18432\n"
    "v_mfma_f32_16x16x32_bf16 v[76:79], a[28:31], a[124:127], v[76:79]\n"
    "buffer_load_dwordx4 v207, s[24:27], s57 offen offset:3072 lds\n"
    "v_mfma_f32_16x16x32_bf16 v[16:19], a[224:227], a[128:131], v[16:19]\n"
    "ds_read_b64_tr_b16 a[8:9], v209 offset:12288\n"
    "v_mfma_f32_16x16x32_bf16 v[16:19], a[228:231], a[144:147], v[16:19]\n"
    "s_add_u32 s57, s57, 25088\n"
    "v_mfma_f32_16x16x32_bf16 v[32:35], a[224:227], a[132:135], v[32:35]\n"
    "ds_read_b64_tr_b16 a[10:11], v209 offset:14336\n"
    "s_add_u32 m0, s78, 4096\n"
    "v_mfma_f32_16x16x32_bf16 v[32:35], a[228:231], a[148:151], v[32:35]\n"
    "buffer_load_dwordx4 v204, s[24:27], s57 offen lds\n"
    "v_mfma_f32_16x16x32_bf16 v[48:51], a[224:227], a[136:139], v[48:51]\n"
    "ds_read_b64_tr_b16 a[12:13], v209 offset:16384\n"
    "v_mfma_f32_16x16x32_bf16 v[48:51], a[228:231], a[152:155], v[48:51]\n"
    "v_mfma_f32_16x16x32_bf16 v[64:67], a[224:227], a[140:143], v[64:67]\n"
    "ds_read_b64_tr_b16 a[14:15], v209 offset:18432\n"
    "v_mfma_f32_16x16x32_bf16 v[64:67], a[228:231], a[156:159], v[64:67]\n"
    "v_mfma_f32_16x16x32_bf16 v[20:23], a[232:235], a[128:131], v[20:23]\n"
    "ds_read_b64_tr_b16 a[16:17], v210 offset:12288\n"
    "v_mfma_f32_16x16x32_bf16 v[20:23], a[236:239], a[144:147], v[20:23]\n"
    "buffer_load_dword v220, v221, s[8:11], s54 offen sc1\n"
    "v_mfma_f32_16x16x32_bf16 v[36:39], a[232:235], a[132:135], v[36:39]\n"
    "ds_read_b64_tr_b16 a[18:19], v210 offset:14336\n"
    "v_mfma_f32_16x16x32_bf16 v[36:39], a[236:239], a[148:151], v[36:39]\n"
    "v_mfma_f32_16x16x32_bf16 v[52:55], a[232:235], a[136:139], v[52:55]\n"
    "ds_read_b64_tr_b16 a[20:21], v210 offset:16384\n"
    "v_mfma_f32_16x16x32_bf16 v[52:55], a[236:239], a[152:155], v[52:55]\n"
    "buffer_load_dwordx4 v205, s[24:27], s57 offen offset:1024 lds\n"
    "v_mfma_f32_16x16x32_bf16 v[68:71], a[232:235], a[140:143], v[68:71]\n"
    "ds_read_b64_tr_b16 a[22:23], v210 offset:18432\n"
    "v_mfma_f32_16x16x32_bf16 v[68:71], a[236:239], a[156:159], v[68:71]\n"
    "v_mfma_f32_16x16x32_bf16 v[24:27], a[240:243], a[128:131], v[24:27]\n"
    "ds_read_b64_tr_b16 a[24:25], v211 offset:12288\n"
    "v_mfma_f32_16x16x32_bf16 v[24:27], a[244:247], a[144:147], v[24:27]\n"
    "buffer_load_dwordx4 v206, s[24:27], s57 offen offset:2048 lds\n"
    "v_mfma_f32_16x16x32_bf16 v[40:43], a[240:243], a[132:135], v[40:43]\n"
    "ds_read_b64_tr_b16 a[26:27], v211 offset:14336\n"
    "v_mfma_f32_16x16x32_bf16 v[40:43], a[244:247], a[148:151], v[40:43]\n"
    "v_mfma_f32_16x16x32_bf16 v[56:59], a[240:243], a[136:139], v[56:59]\n"
    "ds_read_b64_tr_b16 a[28:29], v211 offset:16384\n"
    "s_waitcnt vmcnt(12)\n"
    "v_mfma_f32_16x16x32_bf16 v[56:59], a[244:247], a[152:155], v[56:59]\n"
    "buffer_load_dwordx4 v207, s[24:27], s57 offen offset:3072 lds\n"
    "v_mfma_f32_16x16x32_bf16 v[72:75], a[240:243], a[140:143], v[72:75]\n"
    "ds_read_b64_tr_b16 a[30:31], v211 offset:18432\n"
    "s_add_u32 s57, s57, 25088\n"
    "v_mfma_f32_16x16x32_bf16 v[72:75], a[244:247], a[156:159], v[72:75]\n"
    "s_add_u32 m0, s78, 20480\n"
    "v_mfma_f32_16x16x32_bf16 v[28:31], a[248:251], a[128:131], v[28:31]\n"
    "ds_read_b64_tr_b16 a[224:225], v208 offset:36864\n"
    "s_waitcnt vmcnt(5)\n"
    "v_mfma_f32_16x16x32_bf16 v[28:31], a[252:255], a[144:147], v[28:31]\n"
    "buffer_load_dwordx4 v204, s[24:27], s57 offen lds\n"
    "v_mfma_f32_16x16x32_bf16 v[44:47], a[248:251], a[132:135], v[44:47]\n"
    "ds_read_b64_tr_b16 a[226:227], v208 offset:38912\n"
    "v_mfma_f32_16x16x32_bf16 v[44:47], a[252:255], a[148:151], v[44:47]\n"
    "ds_read_b64_tr_b16 a[32:33], v208 offset:32768\n"
    "v_mfma_f32_16x16x32_bf16 v[60:63], a[248:251], a[136:139], v[60:63]\n"
    "ds_read_b64_tr_b16 a[228:229], v208 offset:28672\n"
    "s_waitcnt lgkmcnt(4)\n"
    "v_mfma_f32_16x16x32_bf16 v[60:63], a[252:255], a[152:155], v[60:63]\n"
    "buffer_load_dwordx4 v205, s[24:27], s57 offen offset:1024 lds\n"
    "v_mfma_f32_16x16x32_bf16 v[76:79], a[248:251], a[140:143], v[76:79]\n"
    "ds_read_b64_tr_b16 a[230:231], v208 offset:30720\n"
    "v_mfma_f32_16x16x32_bf16 v[76:79], a[252:255], a[156:159], v[76:79]\n"
    "ds_read_b64_tr_b16 a[34:35], v208 offset:34816\n"
    "v_mfma_f32_16x16x32_bf16 v[16:19], a[0:3], a[160:163], v[16:19]\n"
    "ds_read_b64_tr_b16 a[232:233], v209 offset:36864\n"
    "v_mfma_f32_16x16x32_bf16 v[16:19], a[4:7], a[176:179], v[16:19]\n"
    "ds_read_b64_tr_b16 a[36:37], v208\n"
    "v_mfma_f32_16x16x32_bf16 v[32:35], a[0:3], a[164:167], v[32:35]\n"
    "ds_read_b64_tr_b16 a[234:235], v209 offset:38912\n"
    "v_mfma_f32_16x16x32_bf16 v[32:35], a[4:7], a[180:183], v[32:35]\n"
    "buffer_load_dwordx4 v206, s[24:27], s57 offen offset:2048 lds\n"
    "v_mfma_f32_16x16x32_bf16 v[48:51], a[0:3], a[168:171], v[48:51]\n"
    "ds_read_b64_tr_b16 a[236:237], v209 offset:28672\n"
    "v_mfma_f32_16x16x32_bf16 v[48:51], a[4:7], a[184:187], v[48:51]\n"
    "ds_read_b64_tr_b16 a[38:39], v208 offset:2048\n"
    "v_mfma_f32_16x16x32_bf16 v[64:67], a[0:3], a[172:175], v[64:67]\n"
    "ds_read_b64_tr_b16 a[238:239], v209 offset:30720\n"
    "v_mfma_f32_16x16x32_bf16 v[64:67], a[4:7], a[188:191], v[64:67]\n"
    "buffer_load_dwordx4 v207, s[24:27], s57 offen offset:3072 lds\n"
    "v_mfma_f32_16x16x32_bf16 v[20:23], a[8:11], a[160:163], v[20:23]\n"
    "ds_read_b64_tr_b16 a[240:241], v210 offset:36864\n"
    "s_waitcnt vmcnt(7)\n"
    "v_mfma_f32_16x16x32_bf16 v[20:23], a[12:15], a[176:179], v[20:23]\n"
    "v_cmp_ne_u32 vcc, 1, v220\n"
    "v_mfma_f32_16x16x32_bf16 v[36:39], a[8:11], a[164:167], v[36:39]\n"
    "ds_read_b64_tr_b16 a[242:243], v210 offset:38912\n"
    "s_cbranch_vccnz .Lo_35_%=\n"
    ".Lr_36_%=:\n"
    "v_mfma_f32_16x16x32_bf16 v[36:39], a[12:15], a[180:183], v[36:39]\n"
    "buffer_load_dwordx4 v[128:131], v10, s[8:11], 0 offen sc1\n"
    "v_mfma_f32_16x16x32_bf16 v[52:55], a[8:11], a[168:171], v[52:55]\n"
    "ds_read_b64_tr_b16 a[244:245], v210 offset:28672\n"
    "v_mfma_f32_16x16x32_bf16 v[52:55], a[12:15], a[184:187], v[52:55]\n"
    "ds_read_b64_tr_b16 a[48:49], v209 offset:32768\n"
    "v_mfma_f32_16x16x32_bf16 v[68:71], a[8:11], a[172:175], v[68:71]\n"
    "ds_read_b64_tr_b16 a[246:247], v210 offset:30720\n"
    "v_mfma_f32_16x16x32_bf16 v[68:71], a[12:15], a[188:191], v[68:71]\n"
    "buffer_load_dwordx4 v[144:147], v10, s[8:11], 0 offen offset:1024 sc1\n"
    "v_mfma_f32_16x16x32_bf16 v[24:27], a[16:19], a[160:163], v[24:27]\n"
    "ds_read_b64_tr_b16 a[248:249], v211 offset:36864\n"
    "v_mfma_f32_16x16x32_bf16 v[24:27], a[20:23], a[176:179], v[24:27]\n"
    "ds_read_b64_tr_b16 a[50:51], v209 offset:34816\n"
    "v_mfma_f32_16x16x32_bf16 v[40:43], a[16:19], a[164:167], v[40:43]\n"
    "ds_read_b64_tr_b16 a[250:251], v211 offset:38912\n"
    "v_mfma_f32_16x16x32_bf16 v[40:43], a[20:23], a[180:183], v[40:43]\n"
    "buffer_load_dwordx4 v[160:163], v10, s[8:11], 0 offen offset:2048 sc1\n"
    "v_mfma_f32_16x16x32_bf16 v[56:59], a[16:19], a[168:171], v[56:59]\n"
    "ds_read_b64_tr_b16 a[252:253], v211 offset:28672\n"
    "v_mfma_f32_16x16x32_bf16 v[56:59], a[20:23], a[184:187], v[56:59]\n"
    "ds_read_b64_tr_b16 a[52:53], v209\n"
    "v_mfma_f32_16x16x32_bf16 v[72:75], a[16:19], a[172:175], v[72:75]\n"
    "ds_read_b64_tr_b16 a[254:255], v211 offset:30720\n"
    "v_mfma_f32_16x16x32_bf16 v[72:75], a[20:23], a[188:191], v[72:75]\n"
    "buffer_load_dwordx4 v[176:179], v10, s[8:11], 0 offen offset:3072 sc1\n"
    "v_mfma_f32_16x16x32_bf16 v[28:31], a[24:27], a[160:163], v[28:31]\n"
    "ds_read_b64_tr_b16 a[54:55], v209 offset:2048\n"
    "v_mfma_f32_16x16x32_bf16 v[28:31], a[28:31], a[176:179], v[28:31]\n"
    "ds_read_b64_tr_b16 a[64:65], v210 offset:32768\n"
    "v_mfma_f32_16x16x32_bf16 v[44:47], a[24:27], a[164:167], v[44:47]\n"
    "ds_read_b64_tr_b16 a[66:67], v210 offset:34816\n"
    "v_mfma_f32_16x16x32_bf16 v[44:47], a[28:31], a[180:183], v[44:47]\n"
    "buffer_load_dwordx4 v[132:135], v11, s[8:11], 0 offen sc1\n"
    "v_mfma_f32_16x16x32_bf16 v[60:63], a[24:27], a[168:171], v[60:63]\n"
    "s_waitcnt lgkmcnt(3)\n"
    "v_mfma_f32_16x16x32_bf16 v[60:63], a[28:31], a[184:187], v[60:63]\n"
    "ds_read_b64_tr_b16 a[68:69], v210\n"
    "v_mfma_f32_16x16x32_bf16 v[76:79], a[24:27], a[172:175], v[76:79]\n"
    "v_mfma_f32_16x16x32_bf16 v[76:79], a[28:31], a[188:191], v[76:79]\n"
    "buffer_load_dwordx4 v[148:151], v11, s[8:11], 0 offen offset:1024 sc1\n"
    "v_mfma_f32_16x16x32_bf16 v[16:19], a[224:227], a[192:195], v[16:19]\n"
    "ds_read_b64_tr_b16 a[70:71], v210 offset:2048\n"
    "v_mfma_f32_16x16x32_bf16 v[16:19], a[228:231], a[208:211], v[16:19]\n"
    "ds_read_b64_tr_b16 a[80:81], v211 offset:32768\n"
    "v_mfma_f32_16x16x32_bf16 v[32:35], a[224:227], a[196:199], v[32:35]\n"
    "ds_read_b64_tr_b16 a[82:83], v211 offset:34816\n"
    "v_mfma_f32_16x16x32_bf16 v[32:35], a[228:231], a[212:215], v[32:35]\n"
    "buffer_load_dwordx4 v[164:167], v11, s[8:11], 0 offen offset:2048 sc1\n"
    "v_mfma_f32_16x16x32_bf16 v[48:51], a[224:227], a[200:203], v[48:51]\n"
    "ds_read_b64_tr_b16 a[84:85], v211\n"
    "v_mfma_f32_16x16x32_bf16 v[48:51], a[228:231], a[216:219], v[48:51]\n"
    "ds_read_b64_tr_b16 a[86:87], v211 offset:2048\n"
    "v_mfma_f32_16x16x32_bf16 v[20:23], a[232:235], a[192:195], v[20:23]\n"
    "s_waitcnt vmcnt(7)\n"
    "v_mfma_f32_16x16x32_bf16 v[20:23], a[236:239], a[208:211], v[20:23]\n"
    "buffer_load_dwordx4 v[180:183], v11, s[8:11], 0 offen offset:3072 sc1\n"
    "v_mfma_f32_16x16x32_bf16 v[36:39], a[232:235], a[196:199], v[36:39]\n"
    "ds_read_b64_tr_b16 a[40:41], v208 offset:4096\n"
    "v_mfma_f32_16x16x32_bf16 v[36:39], a[236:239], a[212:215], v[36:39]\n"
    "ds_read_b64_tr_b16 a[42:43], v208 offset:6144\n"
    "v_mfma_f32_16x16x32_bf16 v[52:55], a[232:235], a[200:203], v[52:55]\n"
    "ds_read_b64_tr_b16 a[44:45], v208 offset:20480\n"
    "v_mfma_f32_16x16x32_bf16 v[52:55], a[236:239], a[216:219], v[52:55]\n"
    "buffer_load_dwordx4 v[136:139], v12, s[8:11], 0 offen sc1\n"
    "v_mfma_f32_16x16x32_bf16 v[24:27], a[240:243], a[192:195], v[24:27]\n"
    "ds_read_b64_tr_b16 a[46:47], v208 offset:22528\n"
    "v_mfma_f32_16x16x32_bf16 v[24:27], a[244:247], a[208:211], v[24:27]\n"
    "ds_read_b64_tr_b16 a[56:57], v209 offset:4096\n"
    "v_mfma_f32_16x16x32_bf16 v[40:43], a[240:243], a[196:199], v[40:43]\n"
    "ds_read_b64_tr_b16 a[58:59], v209 offset:6144\n"
    "v_mfma_f32_16x16x32_bf16 v[40:43], a[244:247], a[212:215], v[40:43]\n"
    "buffer_load_dwordx4 v[152:155], v12, s[8:11], 0 offen offset:1024 sc1\n"
    "v_mfma_f32_16x16x32_bf16 v[56:59], a[240:243], a[200:203], v[56:59]\n"
    "ds_read_b64_tr_b16 a[60:61], v209 offset:20480\n"
    "v_mfma_f32_16x16x32_bf16 v[56:59], a[244:247], a[216:219], v[56:59]\n"
    "ds_read_b64_tr_b16 a[62:63], v209 offset:22528\n"
    "v_mfma_f32_16x16x32_bf16 v[28:31], a[248:251], a[192:195], v[28:31]\n"
    "ds_read_b64_tr_b16 a[72:73], v210 offset:4096\n"
    "v_mfma_f32_16x16x32_bf16 v[28:31], a[252:255], a[208:211], v[28:31]\n"
    "buffer_load_dwordx4 v[168:171], v12, s[8:11], 0 offen offset:2048 sc1\n"
    "v_mfma_f32_16x16x32_bf16 v[44:47], a[248:251], a[196:199], v[44:47]\n"
    "ds_read_b64_tr_b16 a[74:75], v210 offset:6144\n"
    "v_mfma_f32_16x16x32_bf16 v[44:47], a[252:255], a[212:215], v[44:47]\n"
    "ds_read_b64_tr_b16 a[76:77], v210 offset:20480\n"
    "v_mfma_f32_16x16x32_bf16 v[60:63], a[248:251], a[200:203], v[60:63]\n"
    "ds_read_b64_tr_b16 a[78:79], v210 offset:22528\n"
    "v_mfma_f32_16x16x32_bf16 v[60:63], a[252:255], a[216:219], v[60:63]\n"
    "buffer_load_dwordx4 v[184:187], v12, s[8:11], 0 offen offset:3072 sc1\n"
    "s_waitcnt vmcnt(8)\n"
    "s_waitcnt lgkmcnt(8)\n"
    "v_mfma_f32_16x16x32_bf16 v[16:19], a[32:35], v[128:131], v[16:19]\n"
    "ds_read_b64_tr_b16 a[88:89], v211 offset:4096\n"
    "v_mfma_f32_16x16x32_bf16 v[16:19], a[36:39], v[144:147], v[16:19]\n"
    "ds_read_b64_tr_b16 a[90:91], v211 offset:6144\n"
    "v_mfma_f32_16x16x32_bf16 v[16:19], a[40:43], v[160:163], v[16:19]\n"
    "ds_read_b64_tr_b16 a[92:93], v211 offset:20480\n"
    "v_mfma_f32_16x16x32_bf16 v[16:19], a[44:47], v[176:179], v[16:19]\n"
    "buffer_load_dwordx4 v[140:143], v13, s[8:11], 0 offen sc1\n"
    "s_waitcnt lgkmcnt(7)\n"
    "v_mfma_f32_16x16x32_bf16 v[20:23], a[48:51], v[128:131], v[20:23]\n"
    "ds_read_b64_tr_b16 a[94:95], v211 offset:22528\n"
    "v_mfma_f32_16x16x32_bf16 v[20:23], a[52:55], v[144:147], v[20:23]\n"
    "v_mfma_f32_16x16x32_bf16 v[20:23], a[56:59], v[160:163], v[20:23]\n"
    "v_mfma_f32_16x16x32_bf16 v[20:23], a[60:63], v[176:179], v[20:23]\n"
    "buffer_load_dwordx4 v[156:159], v13, s[8:11], 0 offen offset:1024 sc1\n"
    "s_waitcnt lgkmcnt(4)\n"
    "v_mfma_f32_16x16x32_bf16 v[24:27], a[64:67], v[128:131], v[24:27]\n"
    "v_mfma_f32_16x16x32_bf16 v[24:27], a[68:71], v[144:147], v[24:27]\n"
    "v_mfma_f32_16x16x32_bf16 v[24:27], a[72:75], v[160:163], v[24:27]\n"
    "ds_write_b128 v96, v[16:19]\n"
    "v_mfma_f32_16x16x32_bf16 v[24:27], a[76:79], v[176:179], v[24:27]\n"
    "buffer_load_dwordx4 v[172:175], v13, s[8:11], 0 offen offset:2048 sc1\n"
    "s_waitcnt lgkmcnt(1)\n"
    "v_mfma_f32_16x16x32_bf16 v[28:31], a[80:83], v[128:131], v[28:31]\n"
    "v_mfma_f32_16x16x32_bf16 v[28:31], a[84:87], v[144:147], v[28:31]\n"
    "v_mfma_f32_16x16x32_bf16 v[28:31], a[88:91], v[160:163], v[28:31]\n"
    "ds_write_b128 v97, v[20:23]\n"
    "v_mfma_f32_16x16x32_bf16 v[28:31], a[92:95], v[176:179], v[28:31]\n"
    "buffer_load_dwordx4 v[188:191], v13, s[8:11], 0 offen offset:3072 sc1\n"
    "s_waitcnt vmcnt(8)\n"
    "v_mfma_f32_16x16x32_bf16 v[32:35], a[32:35], v[132:135], v[32:35]\n"
    "v_mfma_f32_16x16x32_bf16 v[32:35], a[36:39], v[148:151], v[32:35]\n"
    "v_mfma_f32_16x16x32_bf16 v[32:35], a[40:43], v[164:167], v[32:35]\n"
    "ds_write_b128 v98, v[24:27]\n"
    "v_mfma_f32_16x16x32_bf16 v[32:35], a[44:47], v[180:183], v[32:35]\n"
    "v_mfma_f32_16x16x32_bf16 v[36:39], a[48:51], v[132:135], v[36:39]\n"
    "v_mfma_f32_16x16x32_bf16 v[36:39], a[52:55], v[148:151], v[36:39]\n"
    "v_mfma_f32_16x16x32_bf16 v[36:39], a[56:59], v[164:167], v[36:39]\n"
    "ds_write_b128 v99, v[28:31]\n"
    "v_mfma_f32_16x16x32_bf16 v[36:39], a[60:63], v[180:183], v[36:39]\n"
    "v_mfma_f32_16x16x32_bf16 v[40:43], a[64:67], v[132:135], v[40:43]\n"
    "v_mfma_f32_16x16x32_bf16 v[40:43], a[68:71], v[148:151], v[40:43]\n"
    "v_mfma_f32_16x16x32_bf16 v[40:43], a[72:75], v[164:167], v[40:43]\n"
    "ds_write_b128 v96, v[32:35] offset:4096\n"
    "v_mfma_f32_16x16x32_bf16 v[40:43], a[76:79], v[180:183], v[40:43]\n"
    "v_mfma_f32_16x16x32_bf16 v[44:47], a[80:83], v[132:135], v[44:47]\n"
    "v_mfma_f32_16x16x32_bf16 v[44:47], a[84:87], v[148:151], v[44:47]\n"
    "v_mfma_f32_16x16x32_bf16 v[44:47], a[88:91], v[164:167], v[44:47]\n"
    "ds_write_b128 v97, v[36:39] offset:4096\n"
    "v_mfma_f32_16x16x32_bf16 v[44:47], a[92:95], v[180:183], v[44:47]\n"
    "s_waitcnt vmcnt(4)\n"
    "v_mfma_f32_16x16x32_bf16 v[48:51], a[32:35], v[136:139], v[48:51]\n"
    "v_mfma_f32_16x16x32_bf16 v[48:51], a[36:39], v[152:155], v[48:51]\n"
    "v_mfma_f32_16x16x32_bf16 v[48:51], a[40:43], v[168:171], v[48:51]\n"
    "ds_write_b128 v98, v[40:43] offset:4096\n"
    "v_mfma_f32_16x16x32_bf16 v[48:51], a[44:47], v[184:187], v[48:51]\n"
    "v_mfma_f32_16x16x32_bf16 v[52:55], a[48:51], v[136:139], v[52:55]\n"
    "v_mfma_f32_16x16x32_bf16 v[52:55], a[52:55], v[152:155], v[52:55]\n"
    "v_mfma_f32_16x16x32_bf16 v[52:55], a[56:59], v[168:171], v[52:55]\n"
    "ds_write_b128 v99, v[44:47] offset:4096\n"
    "v_mfma_f32_16x16x32_bf16 v[52:55], a[60:63], v[184:187], v[52:55]\n"
    "v_mfma_f32_16x16x32_bf16 v[56:59], a[64:67], v[136:139], v[56:59]\n"
    "v_mfma_f32_16x16x32_bf16 v[56:59], a[68:71], v[152:155], v[56:59]\n"
    "v_mfma_f32_16x16x32_bf16 v[56:59], a[72:75], v[168:171], v[56:59]\n"
    "ds_write_b128 v96, v[48:51] offset:8192\n"
    "v_mfma_f32_16x16x32_bf16 v[56:59], a[76:79], v[184:187], v[56:59]\n"
    "v_mfma_f32_16x16x32_bf16 v[60:63], a[80:83], v[136:139], v[60:63]\n"
    "v_mfma_f32_16x16x32_bf16 v[60:63], a[84:87], v[152:155], v[60:63]\n"
    "v_mfma_f32_16x16x32_bf16 v[60:63], a[88:91], v[168:171], v[60:63]\n"
    "ds_write_b128 v97, v[52:55] offset:8192\n"
    "v_mfma_f32_16x16x32_bf16 v[60:63], a[92:95], v[184:187], v[60:63]\n"
    "v_mfma_f32_16x16x32_bf16 v[64:67], a[224:227], a[204:207], v[64:67]\n"
    "v_mfma_f32_16x16x32_bf16 v[64:67], a[228:231], a[220:223], v[64:67]\n"
    "s_waitcnt vmcnt(0)\n"
    "v_mfma_f32_16x16x32_bf16 v[64:67], a[32:35], v[140:143], v[64:67]\n"
    "ds_write_b128 v98, v[56:59] offset:8192\n"
    "v_mfma_f32_16x16x32_bf16 v[64:67], a[36:39], v[156:159], v[64:67]\n"
    "v_mfma_f32_16x16x32_bf16 v[64:67], a[40:43], v[172:175], v[64:67]\n"
    "v_mfma_f32_16x16x32_bf16 v[64:67], a[44:47], v[188:191], v[64:67]\n"
    "v_mfma_f32_16x16x32_bf16 v[68:71], a[232:235], a[204:207], v[68:71]\n"
    "ds_write_b128 v99, v[60:63] offset:8192\n"
    "v_mfma_f32_16x16x32_bf16 v[68:71], a[236:239], a[220:223], v[68:71]\n"
    "v_mfma_f32_16x16x32_bf16 v[68:71], a[48:51], v[140:143], v[68:71]\n"
    "v_mfma_f32_16x16x32_bf16 v[68:71], a[52:55], v[156:159], v[68:71]\n"
    "s_waitcnt lgkmcnt(0)\n"
    "v_mfma_f32_16x16x32_bf16 v[68:71], a[56:59], v[172:175], v[68:71]\n"
    "s_barrier\n"
    "v_mfma_f32_16x16x32_bf16 v[68:71], a[60:63], v[188:191], v[68:71]\n"
    "ds_read_b128 v[240:243], v224\n"
    "v_mfma_f32_16x16x32_bf16 v[72:75], a[240:243], a[204:207], v[72:75]\n"
    "ds_write_b128 v96, v[64:67] offset:12288\n"
    "v_mfma_f32_16x16x32_bf16 v[72:75], a[244:247], a[220:223], v[72:75]\n"
    "ds_read_b128 v[244:247], v225\n"
    "v_mfma_f32_16x16x32_bf16 v[72:75], a[64:67], v[140:143], v[72:75]\n"
    "ds_read_b128 v[248:251], v224 offset:2048\n"
    "v_mfma_f32_16x16x32_bf16 v[72:75], a[68:71], v[156:159], v[72:75]\n"
    "ds_read_b128 v[252:255], v225 offset:2048\n"
    "v_mfma_f32_16x16x32_bf16 v[72:75], a[72:75], v[172:175], v[72:75]\n"
    "ds_read_b128 v[112:115], v228 offset:4096\n"
    "v_mfma_f32_16x16x32_bf16 v[72:75], a[76:79], v[188:191], v[72:75]\n"
    "ds_read_b128 v[116:119], v229 offset:4096\n"
    "v_mfma_f32_16x16x32_bf16 v[76:79], a[248:251], a[204:207], v[76:79]\n"
    "ds_write_b128 v97, v[68:71] offset:12288\n"
    "v_mfma_f32_16x16x32_bf16 v[76:79], a[252:255], a[220:223], v[76:79]\n"
    "ds_read_b128 v[120:123], v228 offset:6144\n"
    "v_mfma_f32_16x16x32_bf16 v[76:79], a[80:83], v[140:143], v[76:79]\n"
    "ds_read_b128 v[124:127], v229 offset:6144\n"
    "v_mfma_f32_16x16x32_bf16 v[76:79], a[84:87], v[156:159], v[76:79]\n"
    "ds_read_b128 v[16:19], v232 offset:8192\n"
    "v_mfma_f32_16x16x32_bf16 v[76:79], a[88:91], v[172:175], v[76:79]\n"
    "ds_read_b128 v[20:23], v233 offset:8192\n"
    "v_mfma_f32_16x16x32_bf16 v[76:79], a[92:95], v[188:191], v[76:79]\n"
    "ds_read_b128 v[24:27], v232 offset:10240\n"
    "; ---- merge the four K ranges, add the residual, store\n"
    "ds_read_b128 v[28:31], v233 offset:10240\n"
    "ds_write_b128 v98, v[72:75] offset:12288\n"
    "s_waitcnt lgkmcnt(10)\n"
    "v_pk_add_f32 v[80:81], v[80:81], v[240:241]\n"
    "v_pk_add_f32 v[82:83], v[82:83], v[242:243]\n"
    "v_pk_add_f32 v[84:85], v[84:85], v[244:245]\n"
    "v_pk_add_f32 v[86:87], v[86:87], v[246:247]\n"
    "ds_write_b128 v99, v[76:79] offset:12288\n"
    ".Lpage_8_%=:\n"
    "s_cbranch_execz .Lpage_0_%=\n"
    "ds_read_b128 v[32:35], v236 offset:12288\n"
    "ds_read_b128 v[36:39], v237 offset:12288\n"
    "ds_read_b128 v[40:43], v236 offset:14336\n"
    "ds_read_b128 v[44:47], v237 offset:14336\n"
    "v_pk_add_f32 v[88:89], v[88:89], v[248:249]\n"
    "v_pk_add_f32 v[90:91], v[90:91], v[250:251]\n"
    "v_pk_add_f32 v[92:93], v[92:93], v[252:253]\n"
    "v_pk_add_f32 v[94:95], v[94:95], v[254:255]\n"
    "s_waitcnt lgkmcnt(6)\n"
    "v_pk_add_f32 v[80:81], v[80:81], v[112:113]\n"
    "v_pk_add_f32 v[82:83], v[82:83], v[114:115]\n"
    "v_pk_add_f32 v[84:85], v[84:85], v[116:117]\n"
    "v_pk_add_f32 v[86:87], v[86:87], v[118:119]\n"
    "v_pk_add_f32 v[88:89], v[88:89], v[120:121]\n"
    "v_pk_add_f32 v[90:91], v[90:91], v[122:123]\n"
    "v_pk_add_f32 v[92:93], v[92:93], v[124:125]\n"
    "v_pk_add_f32 v[94:95], v[94:95], v[126:127]\n"
    "v_pk_add_f32 v[80:81], v[80:81], v[16:17]\n"
    "v_pk_add_f32 v[82:83], v[82:83], v[18:19]\n"
    "v_pk_add_f32 v[84:85], v[84:85], v[20:21]\n"
    "v_pk_add_f32 v[86:87], v[86:87], v[22:23]\n"
    "v_pk_add_f32 v[88:89], v[88:89], v[24:25]\n"
    "v_pk_add_f32 v[90:91], v[90:91], v[26:27]\n"
    "v_pk_add_f32 v[92:93], v[92:93], v[28:29]\n"
    "v_pk_add_f32 v[94:95], v[94:95], v[30:31]\n"
    "s_waitcnt lgkmcnt(3)\n"
    "v_pk_add_f32 v[80:81], v[80:81], v[32:33]\n"
    "v_pk_add_f32 v[82:83], v[82:83], v[34:35]\n"
    "v_cvt_pk_bf16_f32 v2, v80, v81\n"
    "v_cvt_pk_bf16_f32 v3, v82, v83\n"
    "s_waitcnt lgkmcnt(2)\n"
    "v_pk_add_f32 v[84:85], v[84:85], v[36:37]\n"
    "v_pk_add_f32 v[86:87], v[86:87], v[38:39]\n"
    "v_cvt_pk_bf16_f32 v4, v84, v85\n"
    "v_cvt_pk_bf16_f32 v5, v86, v87\n"
    "buffer_store_dwordx4 v[2:5], v199, s[28:31], s59 offen sc1\n"
    "s_waitcnt lgkmcnt(1)\n"
    "v_pk_add_f32 v[88:89], v[88:89], v[40:41]\n"
    "v_pk_add_f32 v[90:91], v[90:91], v[42:43]\n"
    "v_cvt_pk_bf16_f32 v6, v88, v89\n"
    "v_cvt_pk_bf16_f32 v7, v90, v91\n"
    "s_waitcnt lgkmcnt(0)\n"
    "v_pk_add_f32 v[92:93], v[92:93], v[44:45]\n"
    "v_pk_add_f32 v[94:95], v[94:95], v[46:47]\n"
    "v_cvt_pk_bf16_f32 v8, v92, v93\n"
    "v_cvt_pk_bf16_f32 v9, v94, v95\n"
    "buffer_store_dwordx4 v[6:9], v199, s[28:31], s62 offen sc1\n"
    "s_endpgm\n"
    ".Lo_35_%=:\n"
    "; bounded re-poll\n"
    "s_mov_b32 s98, 0\n"
    ".Lsp_37_%=:\n"
    "buffer_load_dword v220, v221, s[8:11], s54 offen sc1\n"
    "s_waitcnt vmcnt(0)\n"
    "v_cmp_ne_u32 vcc, 1, v220\n"
    "s_nop 4\n"
    "s_cbranch_vccz .Lsd_38_%=\n"
    "s_add_u32 s98, s98, 1\n"
    "s_cmp_lt_u32 s98, 0x40000\n"
    "s_cbranch_scc1 .Lsp_37_%=\n"
    ".Lsd_38_%=:\n"
    "s_nop 4\n"
    "s_branch .Lr_36_%=\n"
    ".Lo_33_%=:\n"
    "s_waitcnt vmcnt(0)\n"
    "s_barrier\n"
    "buffer_store_dword v219, v216, s[8:11], s66 offen sc1\n"
    "s_endpgm\n"
    "s_branch .Lr_34_%=\n"
    ".Lo_29_%=:\n"
    "; bounded re-poll\n"
    "s_mov_b32 s98, 0\n"
    ".Lsp_31_%=:\n"
    "buffer_load_dword v220, v218, s[8:11], s53 offen sc1\n"
    "s_waitcnt vmcnt(0)\n"
    "v_cmp_ne_u32 vcc, 1, v220\n"
    "s_nop 4\n"
    "s_cbranch_vccz .Lsd_32_%=\n"
    "s_add_u32 s98, s98, 1\n"
    "s_cmp_lt_u32 s98, 0x40000\n"
    "s_cbranch_scc1 .Lsp_31_%=\n"
    ".Lsd_32_%=:\n"
    "s_nop 4\n"
    "s_branch .Lr_30_%=\n"
    ".Lo_19_%=:\n"
    "; bounded re-poll\n"
    "s_mov_b32 s98, 0\n"
    ".Lsp_21_%=:\n"
    "buffer_load_dword v220, v218, s[8:11], s67 offen sc1\n"
    "s_waitcnt vmcnt(0)\n"
    "v_cmp_ne_u32 vcc, 1, v220\n"
    "s_nop 4\n"
    "s_cbranch_vccz .Lsd_22_%=\n"
    "s_add_u32 s98, s98, 1\n"
    "s_cmp_lt_u32 s98, 0x40000\n"
    "s_cbranch_scc1 .Lsp_21_%=\n"
    ".Lsd_22_%=:\n"
    "s_nop 4\n"
    "s_branch .Lr_20_%=\n"
    ".Lo_15_%=:\n"
    "; bounded re-poll\n"
    "s_mov_b32 s98, 0\n"
    ".Lsp_17_%=:\n"
    "buffer_load_dword v220, v221, s[8:11], 0 offen sc1\n"
    "s_waitcnt vmcnt(0)\n"
    "v_cmp_ne_u32 vcc, 1, v220\n"
    "s_nop 4\n"
    "s_cbranch_vccz .Lsd_18_%=\n"
    "s_add_u32 s98, s98, 1\n"
    "s_cmp_lt_u32 s98, 0x40000\n"
    "s_cbranch_scc1 .Lsp_17_%=\n"
    ".Lsd_18_%=:\n"
    "s_nop 4\n"
    "s_branch .Lr_16_%=\n"
    ".Lo_10_%=:\n"
    "; wait for go; count again if the tag came after this workgroup was counted\n"
    "s_mov_b32 s98, 0\n"
    "s_mov_b32 s3, 0\n"
    ".Lgs_12_%=:\n"
    "buffer_load_dword v220, v216, s[8:11], s84 offen sc1\n"
    "s_waitcnt vmcnt(0)\n"
    "v_sub_u32 v220, v220, v201\n"
    "s_nop 0\n"
    "v_readfirstlane_b32 s55, v220\n"
    "s_nop 1\n"
    "s_bitcmp1_b32 s55, 21\n"
    "s_cbranch_scc1 .Lgd_13_%=\n"
    "s_lshr_b32 s55, s55, 20\n"
    "s_andn2_b32 s55, s55, s3\n"
    "s_cbranch_scc0 .Lgn_14_%=\n"
    "s_mov_b32 s3, 1\n"
    "buffer_atomic_add v219, v216, s[8:11], s84 offen\n"
    "s_waitcnt vmcnt(0)\n"
    ".Lgn_14_%=:\n"
    "s_add_u32 s98, s98, 1\n"
    "s_cmp_lt_u32 s98, 0x40000\n"
    "s_cbranch_scc1 .Lgs_12_%=\n"
    ".Lgd_13_%=:\n"
    "s_branch .Lr_11_%=\n"
    ".Lo_1_%=:\n"
    "; bounded re-poll\n"
    "s_mov_b32 s98, 0\n"
    ".Lsp_3_%=:\n"
    "buffer_load_dword v220, v218, s[8:11], 0 offen sc1\n"
    "s_waitcnt vmcnt(0)\n"
    "v_sub_u32 v220, v220, v203\n"
    "v_cmp_ne_u32 vcc, 0x100020, v220\n"
    "s_nop 1\n"
    "s_and_b32 s55, vcc_lo, s67\n"
    "s_nop 4\n"
    "s_cbranch_scc0 .Lsd_4_%=\n"
    "s_add_u32 s98, s98, 1\n"
    "s_cmp_lt_u32 s98, 0x40000\n"
    "s_cbranch_scc1 .Lsp_3_%=\n"
    ".Lsd_4_%=:\n"
    "s_nop 4\n"
    "s_branch .Lr_2_%=\n"
    "; ---- softmax guard, slow path: true row maxima (extra pass over the keys, merged over the waves), subtracted as three bf16 terms\n"
    ".Lfb_26_%=:\n"
    "s_mov_b32 exec_lo, 0\n"
    "v_mov_b32 v230, 0\n"
    "v_mov_b32 v238, 0\n"
    "v_mov_b32 v246, 0\n"
    "v_mov_b32 v254, 0\n"
    "s_mov_b32 exec_lo, -1\n"
    "v_mov_b32 v2, 0xff800000\n"
    "v_mov_b32 v3, 0xff800000\n"
    "v_mov_b32 v4, 0xff800000\n"
    "v_mov_b32 v5, 0xff800000\n"
    "s_mov_b32 s79, s97\n"
    "s_mov_b32 s80, 16\n"
    ".Lfl_28_%=:\n"
    "buffer_load_dwordx4 v[64:67], v1, s[8:11], s79 offen sc1\n"
    "buffer_load_dwordx4 v[68:71], v1, s[8:11], s79 offen offset:1024 sc1\n"
    "s_waitcnt vmcnt(0)\n"
    "v_mfma_f32_32x32x16_bf16 v[80:95], v[64:67], v[224:227], 0\n"
    "v_mfma_f32_32x32x16_bf16 v[80:95], v[68:71], v[228:231], v[80:95]\n"
    "s_nop 11\n"
    "v_max3_f32 v2, v2, v80, v81\n"
    "v_max3_f32 v2, v2, v82, v83\n"
    "v_max3_f32 v2, v2, v84, v85\n"
    "v_max3_f32 v2, v2, v86, v87\n"
    "v_max3_f32 v2, v2, v88, v89\n"
    "v_max3_f32 v2, v2, v90, v91\n"
    "v_max3_f32 v2, v2, v92, v93\n"
    "v_max3_f32 v2, v2, v94, v95\n"
    "v_mfma_f32_32x32x16_bf16 v[80:95], v[64:67], v[232:235], 0\n"
    "v_mfma_f32_32x32x16_bf16 v[80:95], v[68:71], v[236:239], v[80:95]\n"
    "s_nop 11\n"
    "v_max3_f32 v3, v3, v80, v81\n"
    "v_max3_f32 v3, v3, v82, v83\n"
    "v_max3_f32 v3, v3, v84, v85\n"
    "v_max3_f32 v3, v3, v86, v87\n"
    "v_max3_f32 v3, v3, v88, v89\n"
    "v_max3_f32 v3, v3, v90, v91\n"
    "v_max3_f32 v3, v3, v92, v93\n"
    "v_max3_f32 v3, v3, v94, v95\n"
    "v_mfma_f32_32x32x16_bf16 v[80:95], v[64:67], v[240:243], 0\n"
    "v_mfma_f32_32x32x16_bf16 v[80:95], v[68:71], v[244:247], v[80:95]\n"
    "s_nop 11\n"
    "v_max3_f32 v4, v4, v80, v81\n"
    "v_max3_f32 v4, v4, v82, v83\n"
    "v_max3_f32 v4, v4, v84, v85\n"
    "v_max3_f32 v4, v4, v86, v87\n"
    "v_max3_f32 v4, v4, v88, v89\n"
    "v_max3_f32 v4, v4, v90, v91\n"
    "v_max3_f32 v4, v4, v92, v93\n"
    "v_max3_f32 v4, v4, v94, v95\n"
    "v_mfma_f32_32x32x16_bf16 v[80:95], v[64:67], v[248:251], 0\n"
    "v_mfma_f32_32x32x16_bf16 v[80:95], v[68:71], v[252:255], v[80:95]\n"
    "s_nop 11\n"
    "v_max3_f32 v5, v5, v80, v81\n"
    "v_max3_f32 v5, v5, v82, v83\n"
    "v_max3_f32 v5, v5, v84, v85\n"
    "v_max3_f32 v5, v5, v86, v87\n"
    "v_max3_f32 v5, v5, v88, v89\n"
    "v_max3_f32 v5, v5, v90, v91\n"
    "v_max3_f32 v5, v5, v92, v93\n"
    "v_max3_f32 v5, v5, v94, v95\n"
    "s_add_u32 s79, s79, 4096\n"
    "s_sub_u32 s80, s80, 1\n"
    "s_cmp_lg_u32 s80, 0\n"
    "s_cbranch_scc1 .Lfl_28_%=\n"
    "v_mov_b32 v6, v2\n"
    "v_mov_b32 v7, v3\n"
    "v_mov_b32 v8, v4\n"
    "v_mov_b32 v9, v5\n"
    "v_permlane32_swap_b32 v2, v6\n"
    "v_permlane32_swap_b32 v3, v7\n"
    "v_permlane32_swap_b32 v4, v8\n"
    "v_permlane32_swap_b32 v5, v9\n"
    "v_max_f32 v2, v2, v6\n"
    "v_max_f32 v3, v3, v7\n"
    "v_max_f32 v4, v4, v8\n"
    "v_max_f32 v5, v5, v9\n"
    "s_barrier\n"
    "s_lshl_b32 s80, s68, 2\n"
    "v_add_u32 v192, s80, v212\n"
    "ds_write_b32 v192, v2 offset:16384\n"
    "v_add_u32 v192, s80, v213\n"
    "ds_write_b32 v192, v3 offset:16384\n"
    "v_add_u32 v192, s80, v214\n"
    "ds_write_b32 v192, v4 offset:16384\n"
    "v_add_u32 v192, s80, v215\n"
    "ds_write_b32 v192, v5 offset:16384\n"
    "s_waitcnt lgkmcnt(0)\n"
    "s_barrier\n"
    "ds_read_b128 v[80:83], v212 offset:16384\n"
    "ds_read_b128 v[84:87], v213 offset:16384\n"
    "ds_read_b128 v[88:91], v214 offset:16384\n"
    "ds_read_b128 v[92:95], v215 offset:16384\n"
    "s_waitcnt lgkmcnt(0)\n"
    "s_barrier\n"
    "v_max3_f32 v2, v80, v81, v82\n"
    "v_max_f32 v2, v2, v83\n"
    "v_cvt_pk_bf16_f32 v192, 0, -v2\n"
    "v_sub_f32 v193, -v2, v192\n"
    "v_cvt_pk_bf16_f32 v194, 0, v193\n"
    "v_sub_f32 v193, v193, v194\n"
    "s_mov_b32 exec_lo, 0\n"
    "v_cvt_pk_bf16_f32 v230, v192, v194\n"
    "v_cvt_pk_bf16_f32 v231, v193, 0\n"
    "s_mov_b32 exec_lo, -1\n"
    "v_max3_f32 v3, v84, v85, v86\n"
    "v_max_f32 v3, v3, v87\n"
    "v_cvt_pk_bf16_f32 v192, 0, -v3\n"
    "v_sub_f32 v193, -v3, v192\n"
    "v_cvt_pk_bf16_f32 v194, 0, v193\n"
    "v_sub_f32 v193, v193, v194\n"
    "s_mov_b32 exec_lo, 0\n"
    "v_cvt_pk_bf16_f32 v238, v192, v194\n"
    "v_cvt_pk_bf16_f32 v239, v193, 0\n"
    "s_mov_b32 exec_lo, -1\n"
    "v_max3_f32 v4, v88, v89, v90\n"
    "v_max_f32 v4, v4, v91\n"
    "v_cvt_pk_bf16_f32 v192, 0, -v4\n"
    "v_sub_f32 v193, -v4, v192\n"
    "v_cvt_pk_bf16_f32 v194, 0, v193\n"
    "v_sub_f32 v193, v193, v194\n"
    "s_mov_b32 exec_lo, 0\n"
    "v_cvt_pk_bf16_f32 v246, v192, v194\n"
    "v_cvt_pk_bf16_f32 v247, v193, 0\n"
    "s_mov_b32 exec_lo, -1\n"
    "v_max3_f32 v5, v92, v93, v94\n"
    "v_max_f32 v5, v5, v95\n"
    "v_cvt_pk_bf16_f32 v192, 0, -v5\n"
    "v_sub_f32 v193, -v5, v192\n"
    "v_cvt_pk_bf16_f32 v194, 0, v193\n"
    "v_sub_f32 v193, v193, v194\n"
    "s_mov_b32 exec_lo, 0\n"
    "v_cvt_pk_bf16_f32 v254, v192, v194\n"
    "v_cvt_pk_bf16_f32 v255, v193, 0\n"
    "s_mov_b32 exec_lo, -1\n"
    "s_branch .Lfr_27_%=\n"
    ".Lcode_1_%=:\n"

    :
    : "{s[0:1]}"(__builtin_amdgcn_kernarg_segment_ptr()), "{s2}"(blockIdx.x), "{s3}"((unsigned)(size_t)lds), "{v0}"(threadIdx.x)
    : "v1", "v2", "v3", "v4", "v5", "v6", "v7", "v8", "v9", "v10", "v11", "v12", "v13", "v14", "v15", "v16", "v17", "v18", "v19", "v20", "v21", "v22", "v23", "v24", "v25", "v26", "v27", "v28", "v29", "v30", "v31", "v32", "v33", "v34", "v35", "v36", "v37", "v38", "v39", "v40", "v41", "v42", "v43", "v44", "v45", "v46", "v47", "v48", "v49", "v50", "v51", "v52", "v53", "v54", "v55", "v56", "v57", "v58", "v59", "v60", "v61", "v62", "v63", "v64", "v65", "v66", "v67", "v68", "v69", "v70", "v71", "v72", "v73", "v74", "v75", "v76", "v77", "v78", "v79", "v80", "v81", "v82", "v83", "v84", "v85", "v86", "v87", "v88", "v89", "v90", "v91", "v92", "v93", "v94", "v95", "v96", "v97", "v98", "v99", "v100", "v101", "v102", "v103", "v104", "v105", "v106", "v107", "v108", "v109", "v110", "v111", "v112", "v113", "v114", "v115", "v116", "v117", "v118", "v119", "v120", "v121", "v122", "v123", "v124", "v125", "v126", "v127", "v128", "v129", "v130", "v131", "v132", "v133", "v134", "v135", "v136", "v137", "v138", "v139", "v140", "v141", "v142", "v143", "v144", "v145", "v146", "v147", "v148", "v149", "v150", "v151", "v152", "v153", "v154", "v155", "v156", "v157", "v158", "v159", "v160", "v161", "v162", "v163", "v164", "v165", "v166", "v167", "v168", "v169", "v170", "v171", "v172", "v173", "v174", "v175", "v176", "v177", "v178", "v179", "v180", "v181", "v182", "v183", "v184", "v185", "v186", "v187", "v188", "v189", "v190", "v191", "v192", "v193", "v194", "v195", "v196", "v197", "v198", "v199", "v200", "v201", "v202", "v203", "v204", "v205", "v206", "v207", "v208", "v209", "v210", "v211", "v212", "v213", "v214", "v215", "v216", "v217", "v218", "v219", "v220", "v221", "v222", "v223", "v224", "v225", "v226", "v227", "v228", "v229", "v230", "v231", "v232", "v233", "v234", "v235", "v236", "v237", "v238", "v239", "v240", "v241", "v242", "v243", "v244", "v245", "v246", "v247", "v248", "v249", "v250", "v251", "v252", "v253", "v254", "v255", "a0", "a1", "a2", "a3", "a4", "a5", "a6", "a7", "a8", "a9", "a10", "a11", "a12", "a13", "a14", "a15", "a16", "a17", "a18", "a19", "a20", "a21", "a22", "a23", "a24", "a25", "a26", "a27", "a28", "a29", "a30", "a31", "a32", "a33", "a34", "a35", "a36", "a37", "a38", "a39", "a40", "a41", "a42", "a43", "a44", "a45", "a46", "a47", "a48", "a49", "a50", "a51", "a52", "a53", "a54", "a55", "a56", "a57", "a58", "a59", "a60", "a61", "a62", "a63", "a64", "a65", "a66", "a67", "a68", "a69", "a70", "a71", "a72", "a73", "a74", "a75", "a76", "a77", "a78", "a79", "a80", "a81", "a82", "a83", "a84", "a85", "a86", "a87", "a88", "a89", "a90", "a91", "a92", "a93", "a94", "a95", "a96", "a97", "a98", "a99", "a100", "a101", "a102", "a103", "a104", "a105", "a106", "a107", "a108", "a109", "a110", "a111", "a112", "a113", "a114", "a115", "a116", "a117", "a118", "a119", "a120", "a121", "a122", "a123", "a124", "a125", "a126", "a127", "a128", "a129", "a130", "a131", "a132", "a133", "a134", "a135", "a136", "a137", "a138", "a139", "a140", "a141", "a142", "a143", "a144", "a145", "a146", "a147", "a148", "a149", "a150", "a151", "a152", "a153", "a154", "a155", "a156", "a157", "a158", "a159", "a160", "a161", "a162", "a163", "a164", "a165", "a166", "a167", "a168", "a169", "a170", "a171", "a172", "a173", "a174", "a175", "a176", "a177", "a178", "a179", "a180", "a181", "a182", "a183", "a184", "a185", "a186", "a187", "a188", "a189", "a190", "a191", "a192", "a193", "a194", "a195", "a196", "a197", "a198", "a199", "a200", "a201", "a202", "a203", "a204", "a205", "a206", "a207", "a208", "a209", "a210", "a211", "a212", "a213", "a214", "a215", "a216", "a217", "a218", "a219", "a220", "a221", "a222", "a223", "a224", "a225", "a226", "a227", "a228", "a229", "a230", "a231", "a232", "a233", "a234", "a235", "a236", "a237", "a238", "a239", "a240", "a241", "a242", "a243", "a244", "a245", "a246", "a247", "a248", "a249", "a250", "a251", "a252", "a253", "a254", "a255", "s4", "s5", "s6", "s7", "s8", "s9", "s10", "s11", "s12", "s13", "s14", "s15", "s16", "s17", "s18", "s19", "s20", "s21", "s22", "s23", "s24", "s25", "s26", "s27", "s28", "s29", "s30", "s31", "s33", "s34", "s35", "s36", "s37", "s38", "s39", "s40", "s41", "s42", "s43", "s44", "s45", "s46", "s47", "s48", "s49", "s50", "s51", "s52", "s53", "s54", "s55", "s56", "s57", "s58", "s59", "s60", "s61", "s62", "s63", "s64", "s65", "s66", "s67", "s68", "s69", "s70", "s71", "s72", "s73", "s74", "s75", "s76", "s77", "s78", "s79", "s80", "s81", "s82", "s83", "s84", "s85", "s86", "s87", "s88", "s89", "s90", "s91", "s92", "s93", "s94", "s95", "s96", "s97", "s98", "s99", "vcc", "scc", "memory");
}

extern "C" void kernel_launch(void* const* d_in, const int* in_sizes, int n_in, void* d_out, int out_size, void* d_ws, size_t ws_size,
                              hipStream_t stream) {
  const Args args{{d_in[0], d_ws, d_in[8], d_in[4], d_out, d_in[6], d_in[7], d_in[1], d_in[2], d_in[3]}};
  fused_block_kernel<<<256, 256, 0, stream>>>(args);
}
```
